```python
import torch

def _softmax(x, axis=-1):
    return torch.softmax(x, dim=axis)
def _gelu(x):
    return torch.nn.functional.gelu(x, approximate="tanh")
def _silu(x):
    return torch.nn.functional.silu(x)
def _sigmoid(x):
    return torch.sigmoid(x)
def _layernorm(x, axis=-1, eps=1e-5):
    m = x.mean(dim=axis, keepdim=True)
    v = x.var(dim=axis, keepdim=True, unbiased=False)
    return (x - m) / torch.sqrt(v + eps)
def _scatter_add(tgt, idx, src, axis=0):
    return tgt.clone().index_add_(axis, idx, src)
def _segment_sum(src, seg, n):
    out = torch.zeros((n,) + src.shape[1:], dtype=src.dtype)
    return out.index_add_(0, seg, src)
def _pad(x, pw):
    flat = tuple(v for pair in reversed(pw) for v in pair)
    return torch.nn.functional.pad(x, flat)

kernel_name = 'parallel_sum__gqa_block_cached__dense_activation_relu__decode'

def setup_inputs():

    _w = (lambda a: (lambda t: t.to(torch.bfloat16) if t.is_floating_point() else t)(torch.as_tensor(a)))
    def _A_setup():
        B, Sq, D = 1, 1, 448
        Hq, Hkv, Sk, Dh = 16, 8, 32768, 256
        assert Hq % Hkv == 0, (Hq, Hkv)
        return {
            "x": _w((lambda *s: torch.randn(*s, dtype=torch.bfloat16) * 0.05)(B, Sq, D)),
            "k_cache": _w((lambda *s: torch.randn(*s, dtype=torch.bfloat16) * 3.2)(B, Hkv, Sk, Dh)),
            "v_cache": _w((lambda *s: torch.randn(*s, dtype=torch.bfloat16) * 1.6)(B, Hkv, Sk, Dh)),
            "wq": _w((lambda *s: torch.randn(*s, dtype=torch.bfloat16) * 3.2)(D, Hq * Dh) * (D ** -0.5)),
            "wo": _w((lambda *s: torch.randn(*s, dtype=torch.bfloat16) * 1.6)(Hq * Dh, D) * ((Hq * Dh) ** -0.5)),
        }
    _A_in = _A_setup()

    def _B_setup():
        B, S, D, Dff = 1, 1, 448, 1792
        return {
            "x": _w((lambda *s: torch.randn(*s, dtype=torch.bfloat16) * 0.05)(B, S, D)),
            "w1": _w((lambda *s: torch.randn(*s, dtype=torch.bfloat16) * 0.05)(D, Dff)),
            "w2": _w((lambda *s: torch.randn(*s, dtype=torch.bfloat16) * 0.05)(Dff, D)),
        }
    _B_in = _B_setup()

    return {
        "A_x": _A_in["x"],
        "A_k_cache": _A_in["k_cache"],
        "A_v_cache": _A_in["v_cache"],
        "A_wq": _A_in["wq"],
        "A_wo": _A_in["wo"],
        "B_x": _B_in["x"],
        "B_w1": _B_in["w1"],
        "B_w2": _B_in["w2"],
    }

def reference(A_x, A_k_cache, A_v_cache, A_wq, A_wo, B_x, B_w1, B_w2):
    def _A_ref(x, k_cache, v_cache, wq, wo):
        B, Sq, D = x.shape
        Hq, Dh = 16, 256
        Hkv = k_cache.shape[1]
        rep = Hq // Hkv
        q = (lambda x, *d: x.permute(*d))((x @ wq).reshape(B, Sq, Hq, Dh), 0, 2, 1, 3)
        k = (lambda x, n, axis: torch.repeat_interleave(x, n, dim=axis))(k_cache, rep, axis=1)
        v = (lambda x, n, axis: torch.repeat_interleave(x, n, dim=axis))(v_cache, rep, axis=1)
        scores = torch.einsum("bhqd,bhkd->bhqk", q, k) * (Dh ** -0.5)
        p = _softmax(scores, axis=-1)
        o = torch.einsum("bhqk,bhkd->bhqd", p, v)
        return (lambda x, *d: x.permute(*d))(o, 0, 2, 1, 3).reshape(B, Sq, Hq * Dh) @ wo

    def _B_ref(x, w1, w2):
        h = torch.einsum("bsd,df->bsf", x, w1)
        zero = torch.zeros_like(h)
        h = torch.maximum(h, zero)
        return torch.einsum("bsf,fd->bsd", h, w2)

    x = A_x
    xn = _layernorm(x, axis=-1)
    return x + _A_ref(xn, A_k_cache, A_v_cache, A_wq, A_wo) + _B_ref(xn, B_w1, B_w2)
```

```cpp
#include <hip/hip_runtime.h>
#include <hip/hip_bf16.h>
#include <stdint.h>

#define ROUND_S 0

typedef unsigned v4u __attribute__((ext_vector_type(4)));
typedef __bf16 bf16x2 __attribute__((ext_vector_type(2)));
constexpr int D = 448, HQ = 16, HKV = 8, SK = 32768, DH = 256, DFF = 1792;
constexpr int NSPLIT = 32;

__device__ __forceinline__ float bf2f(unsigned short u) { return __uint_as_float(((unsigned)u) << 16); }
__device__ __forceinline__ float rbf(float f) {
    __bf16 b = (__bf16)f; return (float)b;
}
__device__ __forceinline__ unsigned short f2bf(float f) { __bf16 b = (__bf16)f; return __builtin_bit_cast(unsigned short, b); }
__device__ __forceinline__ float dot2(unsigned k, unsigned q, float acc) {
  return __builtin_amdgcn_fdot2_f32_bf16(__builtin_bit_cast(bf16x2, k), __builtin_bit_cast(bf16x2, q), acc, false);
}

__device__ void layernorm448(const unsigned short* x, float* xn  , float* red) {
    int tid = threadIdx.x;
    float s = 0.f;
    for (int i = tid & 63; i < D; i += 64) s += bf2f(x[i]);
    for (int m = 32; m >= 1; m >>= 1) s += __shfl_xor(s, m);
    float mean = s / D;
    float v = 0.f;
    for (int i = tid & 63; i < D; i += 64) { float d = bf2f(x[i]) - mean; v += d * d; }
    for (int m = 32; m >= 1; m >>= 1) v += __shfl_xor(v, m);
    v = v / D;
    float mb = rbf(mean), vb = rbf(v);
    float den = rbf(sqrtf(rbf(vb + 1e-5f)));
    for (int i = tid; i < D; i += blockDim.x) xn[i] = rbf(rbf(bf2f(x[i]) - mb) / den);
    __syncthreads();
}

__global__ void __launch_bounds__(256) k_pre(const unsigned short* x, const unsigned short* wq, const unsigned short* w1,
                                              unsigned short* q, unsigned short* h) {
    __shared__ float xn[D];
    __shared__ float part[32][64];
    layernorm448(x, xn, nullptr);
    int b = blockIdx.x;
    const unsigned short* W; int ld, c0; bool isq = b < 64;
    if (isq) { W = wq; ld = HQ * DH; c0 = b * 64; } else { W = w1; ld = DFF; c0 = (b - 64) * 64; }
    int cg = threadIdx.x & 7, rg = threadIdx.x >> 3;
    float acc[8] = {0,0,0,0,0,0,0,0};
    for (int r = rg; r < D; r += 32) {
        v4u w = *(const v4u*)(W + (size_t)r * ld + c0 + cg * 8);
        float xv = xn[r];
        for (int k = 0; k < 4; ++k) { acc[2*k] += xv * __uint_as_float(w[k] << 16); acc[2*k+1] += xv * __uint_as_float(w[k] & 0xffff0000u); }
    }
    for (int k = 0; k < 8; ++k) part[rg][cg * 8 + k] = acc[k];
    __syncthreads();
    if (threadIdx.x < 64) {
        float s = 0.f;
        for (int r = 0; r < 32; ++r) s += part[r][threadIdx.x];
        if (isq) q[c0 + threadIdx.x] = f2bf(s);
        else { float hv = rbf(s); h[c0 + threadIdx.x] = f2bf(hv > 0.f ? hv : 0.f); }
    }
}

__global__ void __launch_bounds__(256) k_attn(const unsigned short* q, const unsigned short* K, const unsigned short* V, float* pm, float* pl, float* po) {
    int kvh = blockIdx.x & 7, sp = blockIdx.x >> 3;
    int tid = threadIdx.x, lane = tid & 63, wave = tid >> 6, c = lane & 31, hf = lane >> 5;
    __shared__ float sm[4][2][2];
    __shared__ float so[4][2][2][256];
    v4u q0 = *(const v4u*)(q + (kvh * 2 + 0) * DH + c * 8);
    v4u q1 = *(const v4u*)(q + (kvh * 2 + 1) * DH + c * 8);
    const int KEYS = SK / NSPLIT;
    const unsigned short* Kb = K + ((size_t)kvh * SK + (size_t)sp * KEYS) * DH;
    const unsigned short* Vb = V + ((size_t)kvh * SK + (size_t)sp * KEYS) * DH;
    float m0 = -INFINITY, m1 = -INFINITY, l0 = 0.f, l1 = 0.f;
    float a0[8] = {0,0,0,0,0,0,0,0}, a1[8] = {0,0,0,0,0,0,0,0};
    for (int it = wave; it < KEYS / 8; it += 4) {
        v4u kk[4], vv[4];
        for (int t = 0; t < 4; ++t) {
            size_t off = (size_t)(it * 8 + t * 2 + hf) * DH + c * 8;
            kk[t] = __builtin_nontemporal_load((const v4u*)(Kb + off));
            vv[t] = __builtin_nontemporal_load((const v4u*)(Vb + off));
        }
        float s0[4], s1[4];
        for (int t = 0; t < 4; ++t) {
            float d0 = 0.f, d1 = 0.f;
            for (int k = 0; k < 4; ++k) { d0 = dot2(kk[t][k], q0[k], d0); d1 = dot2(kk[t][k], q1[k], d1); }
            for (int m = 16; m >= 1; m >>= 1) { d0 += __shfl_xor(d0, m); d1 += __shfl_xor(d1, m); }
            s0[t] = d0 * 0.0625f; s1[t] = d1 * 0.0625f;
        }
        float n0 = fmaxf(fmaxf(s0[0], s0[1]), fmaxf(s0[2], s0[3])), n1 = fmaxf(fmaxf(s1[0], s1[1]), fmaxf(s1[2], s1[3]));
        n0 = fmaxf(n0, m0); n1 = fmaxf(n1, m1);
        float al0 = __expf(m0 - n0), al1 = __expf(m1 - n1);
        m0 = n0; m1 = n1; l0 *= al0; l1 *= al1;
        for (int k = 0; k < 8; ++k) { a0[k] *= al0; a1[k] *= al1; }
        for (int t = 0; t < 4; ++t) {
            float p0 = __expf(s0[t] - n0), p1 = __expf(s1[t] - n1);
            l0 += p0; l1 += p1;
            for (int k = 0; k < 4; ++k) {
                float lo = __uint_as_float(vv[t][k] << 16), hi = __uint_as_float(vv[t][k] & 0xffff0000u);
                a0[2*k] += p0 * lo; a0[2*k+1] += p0 * hi; a1[2*k] += p1 * lo; a1[2*k+1] += p1 * hi;
            }
        }
    }
    if (c == 0) { sm[wave][hf][0] = m0; sm[wave][hf][1] = m1; }
    __syncthreads();
    float M0 = -INFINITY, M1 = -INFINITY;
    for (int w = 0; w < 4; ++w) for (int h2 = 0; h2 < 2; ++h2) { M0 = fmaxf(M0, sm[w][h2][0]); M1 = fmaxf(M1, sm[w][h2][1]); }
    float f0 = __expf(m0 - M0), f1 = __expf(m1 - M1);
    for (int k = 0; k < 8; ++k) { so[wave][hf][0][c * 8 + k] = a0[k] * f0; so[wave][hf][1][c * 8 + k] = a1[k] * f1; }
    __syncthreads();
    if (c == 0) { sm[wave][hf][0] = l0 * f0; sm[wave][hf][1] = l1 * f1; }
    __syncthreads();
    for (int hh = 0; hh < 2; ++hh) {
        float s = 0.f;
        for (int w = 0; w < 4; ++w) for (int h2 = 0; h2 < 2; ++h2) s += so[w][h2][hh][tid];
        po[((size_t)(kvh * 2 + hh) * NSPLIT + sp) * DH + tid] = s;
        if (tid == 0) {
            float L = 0.f;
            for (int w = 0; w < 4; ++w) for (int h2 = 0; h2 < 2; ++h2) L += sm[w][h2][hh];
            pm[(kvh * 2 + hh) * NSPLIT + sp] = hh == 0 ? M0 : M1;
            pl[(kvh * 2 + hh) * NSPLIT + sp] = L;
        }
    }
}

__global__ void __launch_bounds__(256) k_comb(const float* pm, const float* pl, const float* po, unsigned short* o) {
    int h = blockIdx.x, tid = threadIdx.x;
    float M = -INFINITY;
    for (int s = 0; s < NSPLIT; ++s) M = fmaxf(M, pm[h * NSPLIT + s]);
    float L = 0.f, acc = 0.f;
    for (int s = 0; s < NSPLIT; ++s) { float w = __expf(pm[h * NSPLIT + s] - M); L += w * pl[h * NSPLIT + s]; acc += w * po[((size_t)h * NSPLIT + s) * DH + tid]; }
    o[h * DH + tid] = f2bf(acc / L);
}

__global__ void __launch_bounds__(256) k_out(const unsigned short* x, const unsigned short* o, const unsigned short* h, const unsigned short* wo, const unsigned short* w2, unsigned short* out) {
    __shared__ float part[256][9];
    __shared__ float part2[256][9];
    int c0 = blockIdx.x * 8, tid = threadIdx.x;
    float acc[8] = {0,0,0,0,0,0,0,0}, acc2[8] = {0,0,0,0,0,0,0,0};
    for (int r = tid; r < HQ * DH; r += 256) {
        v4u w = *(const v4u*)(wo + (size_t)r * D + c0); float xv = bf2f(o[r]);
        for (int k = 0; k < 4; ++k) { acc[2*k] += xv * __uint_as_float(w[k] << 16); acc[2*k+1] += xv * __uint_as_float(w[k] & 0xffff0000u); }
    }
    for (int r = tid; r < DFF; r += 256) {
        v4u w = *(const v4u*)(w2 + (size_t)r * D + c0); float xv = bf2f(h[r]);
        for (int k = 0; k < 4; ++k) { acc2[2*k] += xv * __uint_as_float(w[k] << 16); acc2[2*k+1] += xv * __uint_as_float(w[k] & 0xffff0000u); }
    }
    for (int k = 0; k < 8; ++k) { part[tid][k] = acc[k]; part2[tid][k] = acc2[k]; }
    __syncthreads();
    if (tid < 8) {
        float s = 0.f, s2 = 0.f;
        for (int r = 0; r < 256; ++r) { s += part[r][tid]; s2 += part2[r][tid]; }
        float a = rbf(s), f = rbf(s2);
        float xv = bf2f(x[c0 + tid]);
        out[c0 + tid] = f2bf(rbf(xv + a) + f);
    }
}

extern "C" void kernel_launch(void* const* d_in, const int* in_sizes, int n_in,
                              void* d_out, int out_size, void* d_ws, size_t ws_size,
                              hipStream_t stream) {
    const unsigned short* x = (const unsigned short*)d_in[0];
    const unsigned short* K = (const unsigned short*)d_in[1];
    const unsigned short* V = (const unsigned short*)d_in[2];
    const unsigned short* wq = (const unsigned short*)d_in[3];
    const unsigned short* wo = (const unsigned short*)d_in[4];
    const unsigned short* w1 = (const unsigned short*)d_in[6];
    const unsigned short* w2 = (const unsigned short*)d_in[7];
    char* ws = (char*)d_ws;
    unsigned short* q = (unsigned short*)(ws);
    unsigned short* h = (unsigned short*)(ws + 16384);
    unsigned short* o = (unsigned short*)(ws + 32768);
    float* pm = (float*)(ws + 65536);
    float* pl = (float*)(ws + 65536 + 4096);
    float* po = (float*)(ws + 131072);
    k_pre<<<64 + 28, 256, 0, stream>>>(x, wq, w1, q, h);
    k_attn<<<256, 256, 0, stream>>>(q, K, V, pm, pl, po);
    k_comb<<<16, 256, 0, stream>>>(pm, pl, po, o);
    k_out<<<56, 256, 0, stream>>>(x, o, h, wo, w2, (unsigned short*)d_out);
}
```

```cpp
#include <hip/hip_runtime.h>
#include <hip/hip_bf16.h>
#include <stdint.h>

#define ROUND_S 1

typedef unsigned v4u __attribute__((ext_vector_type(4)));
typedef __bf16 bf16x2 __attribute__((ext_vector_type(2)));
constexpr int D = 448, HQ = 16, HKV = 8, SK = 32768, DH = 256, DFF = 1792;
constexpr int NSPLIT = 32;

__device__ __forceinline__ float bf2f(unsigned short u) { return __uint_as_float(((unsigned)u) << 16); }
__device__ __forceinline__ float rbf(float f) {
    __bf16 b = (__bf16)f; return (float)b;
}
__device__ __forceinline__ unsigned short f2bf(float f) { __bf16 b = (__bf16)f; return __builtin_bit_cast(unsigned short, b); }
__device__ __forceinline__ float dot2(unsigned k, unsigned q, float acc) {
  return __builtin_amdgcn_fdot2_f32_bf16(__builtin_bit_cast(bf16x2, k), __builtin_bit_cast(bf16x2, q), acc, false);
}

__device__ void layernorm448(const unsigned short* x, float* xn  , float* red) {
    int tid = threadIdx.x;
    float s = 0.f;
    for (int i = tid & 63; i < D; i += 64) s += bf2f(x[i]);
    for (int m = 32; m >= 1; m >>= 1) s += __shfl_xor(s, m);
    float mean = s / D;
    float v = 0.f;
    for (int i = tid & 63; i < D; i += 64) { float d = bf2f(x[i]) - mean; v += d * d; }
    for (int m = 32; m >= 1; m >>= 1) v += __shfl_xor(v, m);
    v = v / D;
    float mb = rbf(mean), vb = rbf(v);
    float den = rbf(sqrtf(rbf(vb + 1e-5f)));
    for (int i = tid; i < D; i += blockDim.x) xn[i] = rbf(rbf(bf2f(x[i]) - mb) / den);
    __syncthreads();
}

__global__ void __launch_bounds__(256) k_pre(const unsigned short* x, const unsigned short* wq, const unsigned short* w1,
                                              unsigned short* q, unsigned short* h) {
    __shared__ float xn[D];
    __shared__ float part[32][64];
    layernorm448(x, xn, nullptr);
    int b = blockIdx.x;
    const unsigned short* W; int ld, c0; bool isq = b < 64;
    if (isq) { W = wq; ld = HQ * DH; c0 = b * 64; } else { W = w1; ld = DFF; c0 = (b - 64) * 64; }
    int cg = threadIdx.x & 7, rg = threadIdx.x >> 3;
    float acc[8] = {0,0,0,0,0,0,0,0};
    for (int r = rg; r < D; r += 32) {
        v4u w = *(const v4u*)(W + (size_t)r * ld + c0 + cg * 8);
        float xv = xn[r];
        for (int k = 0; k < 4; ++k) { acc[2*k] += xv * __uint_as_float(w[k] << 16); acc[2*k+1] += xv * __uint_as_float(w[k] & 0xffff0000u); }
    }
    for (int k = 0; k < 8; ++k) part[rg][cg * 8 + k] = acc[k];
    __syncthreads();
    if (threadIdx.x < 64) {
        float s = 0.f;
        for (int r = 0; r < 32; ++r) s += part[r][threadIdx.x];
        if (isq) q[c0 + threadIdx.x] = f2bf(s);
        else { float hv = rbf(s); h[c0 + threadIdx.x] = f2bf(hv > 0.f ? hv : 0.f); }
    }
}

constexpr int NW = 8;
__global__ void __launch_bounds__(NW * 64) k_attn(const unsigned short* q, const unsigned short* K, const unsigned short* V, float* pm, float* pl, float* po) {
    int kvh = blockIdx.x & 7, sp = blockIdx.x >> 3;
    int tid = threadIdx.x, lane = tid & 63, wave = __builtin_amdgcn_readfirstlane(tid >> 6), c = lane & 31, hf = lane >> 5;
    __shared__ float sm[NW][2][2];
    __shared__ float so[NW][2][2][256];
    const int KEYS = SK / NSPLIT;
    const unsigned short* Kb = K + ((size_t)kvh * SK + (size_t)sp * KEYS) * DH;
    const unsigned short* Vb = V + ((size_t)kvh * SK + (size_t)sp * KEYS) * DH;
    v4u kn[4], vn[4];
    auto issueK = [&](int it) { for (int t = 0; t < 4; ++t) kn[t] = __builtin_nontemporal_load((const v4u*)(Kb + (size_t)(it * 8 + t * 2 + hf) * DH + c * 8)); };
    auto issueV = [&](int it) { for (int t = 0; t < 4; ++t) vn[t] = __builtin_nontemporal_load((const v4u*)(Vb + (size_t)(it * 8 + t * 2 + hf) * DH + c * 8)); };
    issueK(wave); issueV(wave);
    v4u q0 = *(const v4u*)(q + (kvh * 2 + 0) * DH + c * 8);
    v4u q1 = *(const v4u*)(q + (kvh * 2 + 1) * DH + c * 8);
    float m0 = -INFINITY, m1 = -INFINITY, l0 = 0.f, l1 = 0.f;
    float a0[8] = {0,0,0,0,0,0,0,0}, a1[8] = {0,0,0,0,0,0,0,0};
    for (int it = wave; it < KEYS / 8; it += NW) {
        float s0[4], s1[4];
        int nx = it + NW < KEYS / 8 ? it + NW : it;
        for (int t = 0; t < 4; ++t) {
            float d0 = 0.f, d1 = 0.f;
            for (int k = 0; k < 4; ++k) { d0 = dot2(kn[t][k], q0[k], d0); d1 = dot2(kn[t][k], q1[k], d1); }
            s0[t] = d0; s1[t] = d1;
        }
        issueK(nx);
        for (int t = 0; t < 4; ++t) {
            float d0 = s0[t], d1 = s1[t];
            for (int m = 16; m >= 1; m >>= 1) { d0 += __shfl_xor(d0, m); d1 += __shfl_xor(d1, m); }
            d0 = rbf(d0); d1 = rbf(d1);
            s0[t] = d0 * 0.0625f; s1[t] = d1 * 0.0625f;
        }
        float n0 = fmaxf(fmaxf(s0[0], s0[1]), fmaxf(s0[2], s0[3])), n1 = fmaxf(fmaxf(s1[0], s1[1]), fmaxf(s1[2], s1[3]));
        n0 = fmaxf(n0, m0); n1 = fmaxf(n1, m1);
        float al0 = __expf(m0 - n0), al1 = __expf(m1 - n1);
        m0 = n0; m1 = n1; l0 *= al0; l1 *= al1;
        for (int k = 0; k < 8; ++k) { a0[k] *= al0; a1[k] *= al1; }
        for (int t = 0; t < 4; ++t) {
            float p0 = __expf(s0[t] - n0), p1 = __expf(s1[t] - n1);
            l0 += p0; l1 += p1;
            for (int k = 0; k < 4; ++k) {
                float lo = __uint_as_float(vn[t][k] << 16), hi = __uint_as_float(vn[t][k] & 0xffff0000u);
                a0[2*k] += p0 * lo; a0[2*k+1] += p0 * hi; a1[2*k] += p1 * lo; a1[2*k+1] += p1 * hi;
            }
        }
        issueV(nx);
    }
    if (c == 0) { sm[wave][hf][0] = m0; sm[wave][hf][1] = m1; }
    __syncthreads();
    float M0 = -INFINITY, M1 = -INFINITY;
    for (int w = 0; w < NW; ++w) for (int h2 = 0; h2 < 2; ++h2) { M0 = fmaxf(M0, sm[w][h2][0]); M1 = fmaxf(M1, sm[w][h2][1]); }
    float f0 = __expf(m0 - M0), f1 = __expf(m1 - M1);
    for (int k = 0; k < 8; ++k) { so[wave][hf][0][c * 8 + k] = a0[k] * f0; so[wave][hf][1][c * 8 + k] = a1[k] * f1; }
    __syncthreads();
    if (c == 0) { sm[wave][hf][0] = l0 * f0; sm[wave][hf][1] = l1 * f1; }
    __syncthreads();
    {
        int hh = tid >> 8, dd = tid & 255;
        float s = 0.f;
        for (int w = 0; w < NW; ++w) for (int h2 = 0; h2 < 2; ++h2) s += so[w][h2][hh][dd];
        po[((size_t)(kvh * 2 + hh) * NSPLIT + sp) * DH + dd] = s;
        if (dd == 0) {
            float L = 0.f;
            for (int w = 0; w < NW; ++w) for (int h2 = 0; h2 < 2; ++h2) L += sm[w][h2][hh];
            pm[(kvh * 2 + hh) * NSPLIT + sp] = hh == 0 ? M0 : M1;
            pl[(kvh * 2 + hh) * NSPLIT + sp] = L;
        }
    }
}

__global__ void __launch_bounds__(256) k_comb(const float* pm, const float* pl, const float* po, unsigned short* o) {
    int h = blockIdx.x, tid = threadIdx.x;
    float M = -INFINITY;
    for (int s = 0; s < NSPLIT; ++s) M = fmaxf(M, pm[h * NSPLIT + s]);
    float L = 0.f, acc = 0.f;
    for (int s = 0; s < NSPLIT; ++s) { float w = __expf(pm[h * NSPLIT + s] - M); L += w * pl[h * NSPLIT + s]; acc += w * po[((size_t)h * NSPLIT + s) * DH + tid]; }
    o[h * DH + tid] = f2bf(acc / L);
}

__global__ void __launch_bounds__(256) k_out(const unsigned short* x, const unsigned short* o, const unsigned short* h, const unsigned short* wo, const unsigned short* w2, unsigned short* out) {
    __shared__ float part[256][9];
    __shared__ float part2[256][9];
    int c0 = blockIdx.x * 8, tid = threadIdx.x;
    float acc[8] = {0,0,0,0,0,0,0,0}, acc2[8] = {0,0,0,0,0,0,0,0};
    for (int r = tid; r < HQ * DH; r += 256) {
        v4u w = *(const v4u*)(wo + (size_t)r * D + c0); float xv = bf2f(o[r]);
        for (int k = 0; k < 4; ++k) { acc[2*k] += xv * __uint_as_float(w[k] << 16); acc[2*k+1] += xv * __uint_as_float(w[k] & 0xffff0000u); }
    }
    for (int r = tid; r < DFF; r += 256) {
        v4u w = *(const v4u*)(w2 + (size_t)r * D + c0); float xv = bf2f(h[r]);
        for (int k = 0; k < 4; ++k) { acc2[2*k] += xv * __uint_as_float(w[k] << 16); acc2[2*k+1] += xv * __uint_as_float(w[k] & 0xffff0000u); }
    }
    for (int k = 0; k < 8; ++k) { part[tid][k] = acc[k]; part2[tid][k] = acc2[k]; }
    __syncthreads();
    if (tid < 8) {
        float s = 0.f, s2 = 0.f;
        for (int r = 0; r < 256; ++r) { s += part[r][tid]; s2 += part2[r][tid]; }
        float a = rbf(s), f = rbf(s2);
        float xv = bf2f(x[c0 + tid]);
        out[c0 + tid] = f2bf(rbf(xv + a) + f);
    }
}

extern "C" void kernel_launch(void* const* d_in, const int* in_sizes, int n_in,
                              void* d_out, int out_size, void* d_ws, size_t ws_size,
                              hipStream_t stream) {
    const unsigned short* x = (const unsigned short*)d_in[0];
    const unsigned short* K = (const unsigned short*)d_in[1];
    const unsigned short* V = (const unsigned short*)d_in[2];
    const unsigned short* wq = (const unsigned short*)d_in[3];
    const unsigned short* wo = (const unsigned short*)d_in[4];
    const unsigned short* w1 = (const unsigned short*)d_in[6];
    const unsigned short* w2 = (const unsigned short*)d_in[7];
    char* ws = (char*)d_ws;
    unsigned short* q = (unsigned short*)(ws);
    unsigned short* h = (unsigned short*)(ws + 16384);
    unsigned short* o = (unsigned short*)(ws + 32768);
    float* pm = (float*)(ws + 65536);
    float* pl = (float*)(ws + 65536 + 4096);
    float* po = (float*)(ws + 131072);
    k_pre<<<64 + 28, 256, 0, stream>>>(x, wq, w1, q, h);
    k_attn<<<256, NW * 64, 0, stream>>>(q, K, V, pm, pl, po);
    k_comb<<<16, 256, 0, stream>>>(pm, pl, po, o);
    k_out<<<56, 256, 0, stream>>>(x, o, h, wo, w2, (unsigned short*)d_out);
}
```

```cpp
#include <hip/hip_runtime.h>
#include <hip/hip_bf16.h>
#include <stdint.h>
#include <stdio.h>

#define GAS __attribute__((address_space(1)))
#define LAS __attribute__((address_space(3)))

#define NFLY 5
#define KPOL " nt"
#define VPOL " nt"
#define WNT 1
#define WPOL " nt"

#define WEARLY 0
#define FSLEEP 3
#define QDELAY 60
#define MLFIRST 8
#define QPOLL 1
#define QSLEEP 4
#define PAT 3
#define B0MODE 0
#define TAILG1 gather<8, 1>
#define TAILG2 gather<2, 1>

namespace {

typedef unsigned short u16;
typedef unsigned v4u __attribute__((ext_vector_type(4)));
typedef unsigned v2u __attribute__((ext_vector_type(2)));
typedef float v4f __attribute__((ext_vector_type(4)));
typedef short v4s __attribute__((ext_vector_type(4)));
typedef short v8s __attribute__((ext_vector_type(8)));
typedef __bf16 v8bf __attribute__((ext_vector_type(8)));
typedef __bf16 bf16x2 __attribute__((ext_vector_type(2)));

constexpr int D = 448, HKV = 8, SK = 32768, DH = 256, DFF = 1792, NQ = 4096;
constexpr int NWG = 256, NTHR = 512;
constexpr int NSPLIT = 32;
constexpr int NSW = 4;
constexpr int TILES = SK / NSPLIT / NSW / 16;
constexpr int NFFN = DFF / 16;
constexpr int NRED = D / 8;
constexpr float SC2 = 0.0625f * 1.4426950408889634f;
constexpr unsigned SPIN_CAP = 1u << 18;

constexpr unsigned WS_EPOCH = 0;
constexpr unsigned WS_QG = 4096;
constexpr unsigned WS_ML = WS_QG + 8 * 256 * 8;
constexpr unsigned WS_AG = WS_ML + 16 * 32 * 16;
constexpr unsigned WS_OG = WS_AG + 256 * 4096;
constexpr unsigned WS_FG = WS_OG + NRED * 256 * 64;
constexpr unsigned WS_END = WS_FG + NRED * NFFN * 64;

constexpr unsigned L_RING = 0;
constexpr unsigned L_Q = 131072;
constexpr unsigned L_XN = L_Q + 1024;
constexpr unsigned L_W = L_XN + 896;
constexpr unsigned L_W1 = L_W + 14336;
constexpr unsigned L_O = L_W1 + 14336;
constexpr unsigned L_RED = L_O + 32;
constexpr unsigned L_FX = L_RED + 256;
constexpr unsigned L_TAG = L_FX + 64;
constexpr unsigned L_END = L_TAG + 16;

__device__ __forceinline__ float bflo(unsigned w) { return __uint_as_float(w << 16); }
__device__ __forceinline__ float bfhi(unsigned w) { return __uint_as_float(w & 0xffff0000u); }
__device__ __forceinline__ float rbf(float f) { return (float)(__bf16)f; }
__device__ __forceinline__ unsigned pk2(float a, float b) { bf16x2 v = {(__bf16)a, (__bf16)b}; return __builtin_bit_cast(unsigned, v); }
__device__ __forceinline__ float dot2(unsigned a, unsigned b, float c) {
    return __builtin_amdgcn_fdot2_f32_bf16(__builtin_bit_cast(bf16x2, a), __builtin_bit_cast(bf16x2, b), c, false);
}
template <int CTRL> __device__ __forceinline__ float dpp(float x) {
    return __builtin_bit_cast(float, __builtin_amdgcn_mov_dpp(__builtin_bit_cast(int, x), CTRL, 0xf, 0xf, true));
}
constexpr int XOR1 = 0xB1, XOR2 = 0x4E, ROR4 = 0x124, ROR8 = 0x128;
__device__ __forceinline__ float sum16(float x) {
    auto s = __builtin_amdgcn_permlane16_swap(__float_as_uint(x), __float_as_uint(x), false, false);
    return __uint_as_float(s[0]) + __uint_as_float(s[1]);
}
__device__ __forceinline__ float sum32(float x) {
    auto s = __builtin_amdgcn_permlane32_swap(__float_as_uint(x), __float_as_uint(x), false, false);
    return __uint_as_float(s[0]) + __uint_as_float(s[1]);
}
__device__ __forceinline__ float max16(float x) {
    auto s = __builtin_amdgcn_permlane16_swap(__float_as_uint(x), __float_as_uint(x), false, false);
    return fmaxf(__uint_as_float(s[0]), __uint_as_float(s[1]));
}
__device__ __forceinline__ float max32(float x) {
    auto s = __builtin_amdgcn_permlane32_swap(__float_as_uint(x), __float_as_uint(x), false, false);
    return fmaxf(__uint_as_float(s[0]), __uint_as_float(s[1]));
}
__device__ __forceinline__ float wave_sum(float x) {
    x += dpp<XOR1>(x); x += dpp<XOR2>(x); x += dpp<ROR4>(x); x += dpp<ROR8>(x);
    return sum32(sum16(x));
}
__device__ __forceinline__ float ex2(float x) { return __builtin_amdgcn_exp2f(x); }

#define WG_BARRIER() do { asm volatile("s_waitcnt lgkmcnt(0)" ::: "memory"); __builtin_amdgcn_s_barrier(); asm volatile("" ::: "memory"); } while (0)
#define VMCNT(n) asm volatile("s_waitcnt vmcnt(%0)" :: "i"(n) : "memory")

__device__ __forceinline__ void dma_nt(unsigned ldsaddr, unsigned voff, const void* gbase) {
    asm volatile("s_waitcnt vmcnt(%3)\n\ts_mov_b32 m0, %0\n\ts_nop 0\n\tglobal_load_lds_dwordx4 %1, %2 nt" :: "s"(ldsaddr), "v"(voff), "s"(gbase), "i"(NFLY - 1) : "memory");
}
__device__ __forceinline__ void dma_df(unsigned ldsaddr, unsigned voff, const void* gbase) {
    asm volatile("s_waitcnt vmcnt(%3)\n\ts_mov_b32 m0, %0\n\ts_nop 0\n\tglobal_load_lds_dwordx4 %1, %2" :: "s"(ldsaddr), "v"(voff), "s"(gbase), "i"(NFLY - 1) : "memory");
}
#define KDEF(h) false
#define VDEF(h) false
#define dmaK(a, v, g) do { if (kdef) dma_df(a, v, g); else dma_nt(a, v, g); } while (0)
#define dmaV(a, v, g) do { if (vdef) dma_df(a, v, g); else dma_nt(a, v, g); } while (0)
template <typename T> __device__ __forceinline__ T wload(const T* p) {
    return __builtin_nontemporal_load((const GAS T*)p);
}

typedef __amdgpu_buffer_rsrc_t rsrc_t;
__device__ __forceinline__ v4u ld16_sc1(rsrc_t r, unsigned off) { return __builtin_amdgcn_raw_buffer_load_b128(r, off, 0, 16); }
__device__ __forceinline__ void st16_sc1(rsrc_t r, unsigned off, v4u v) { __builtin_amdgcn_raw_buffer_store_b128(v, r, off, 0, 16); }
__device__ __forceinline__ void st8_sc1(rsrc_t r, unsigned off, v2u v) { __builtin_amdgcn_raw_buffer_store_b64(v, r, off, 0, 16); }

template <int N, int SLEEP> __device__ __forceinline__ void gather(rsrc_t r, const unsigned (&off)[N], unsigned tag, v4u (&v)[N]) {
    for (unsigned spin = 0; spin < SPIN_CAP; ++spin) {
        asm volatile("" ::: "memory");
        bool ok = true;
#pragma unroll
        for (int k = 0; k < N; ++k) v[k] = ld16_sc1(r, off[k]);
#pragma unroll
        for (int k = 0; k < N; ++k) ok &= (v[k].y == tag) & (v[k].w == tag);
        if (__all(ok)) break;
        __builtin_amdgcn_s_sleep(SLEEP);
    }
}

template <int N> __device__ __forceinline__ void gather2(rsrc_t r, const unsigned (&off)[N], unsigned tag, v4u (&v)[N]) {
    v4u a[N], c[N];
#pragma unroll
    for (int k = 0; k < N; ++k) a[k] = ld16_sc1(r, off[k]);
    for (unsigned spin = 0; spin < SPIN_CAP; ++spin) {
        asm volatile("" ::: "memory");
#pragma unroll
        for (int k = 0; k < N; ++k) c[k] = ld16_sc1(r, off[k]);
        asm volatile("" ::: "memory");
        bool ok = true;
#pragma unroll
        for (int k = 0; k < N; ++k) ok &= (a[k].y == tag) & (a[k].w == tag);
        if (__all(ok)) {
#pragma unroll
            for (int k = 0; k < N; ++k) v[k] = a[k];
            return;
        }
        asm volatile("" ::: "memory");
#pragma unroll
        for (int k = 0; k < N; ++k) a[k] = ld16_sc1(r, off[k]);
        asm volatile("" ::: "memory");
        ok = true;
#pragma unroll
        for (int k = 0; k < N; ++k) ok &= (c[k].y == tag) & (c[k].w == tag);
        if (__all(ok)) {
#pragma unroll
            for (int k = 0; k < N; ++k) v[k] = c[k];
            return;
        }
    }
#pragma unroll
    for (int k = 0; k < N; ++k) v[k] = a[k];
}

__device__ __forceinline__ void layernorm_to_lds(v4u xv, LAS char* xn, int lane) {
    float f[8];
#pragma unroll
    for (int k = 0; k < 4; ++k) { f[2 * k] = bflo(xv[k]); f[2 * k + 1] = bfhi(xv[k]); }
    float s = 0.f;
#pragma unroll
    for (int k = 0; k < 8; ++k) s += f[k];
    const float mean = wave_sum(s) * (1.0f / D);
    float v = 0.f;
#pragma unroll
    for (int k = 0; k < 8; ++k) { const float d = f[k] - mean; v = fmaf(d, d, v); }
    if (lane >= 56) v = 0.f;
    const float var = wave_sum(v) * (1.0f / D);
    const float mb = rbf(mean), den = rbf(sqrtf(rbf(rbf(var) + 1e-5f)));
    v4u o;
#pragma unroll
    for (int k = 0; k < 4; ++k) o[k] = pk2(rbf(f[2 * k] - mb) / den, rbf(f[2 * k + 1] - mb) / den);
    if (lane < 56) *(LAS v4u*)(xn + lane * 16) = o;
}

__device__ __forceinline__ void slice_dma(const u16* w, int ld, unsigned ldsaddr, int lane) {
    const unsigned voff = ((unsigned)(lane >> 1) * ld + (lane & 1) * 8) * 2;
#pragma unroll
    for (int i = 0; i < 14; ++i)
        asm volatile("s_mov_b32 m0, %0\n\ts_nop 0\n\tglobal_load_lds_dwordx4 %1, %2" WPOL :: "s"(ldsaddr + i * 1024), "v"(voff), "s"((const char*)w + (size_t)i * 32 * ld * 2) : "memory");
}
__device__ __forceinline__ float slice_mfma(LAS char* L, unsigned wofs, int lane) {
    const int g = lane >> 4, i = lane & 15;
    v4f d = {0.f, 0.f, 0.f, 0.f};
    LAS char* a = L + L_XN + g * 16;
    LAS char* bp = L + wofs + (8 * g + (i >> 2)) * 32 + (i & 3) * 8;
#pragma unroll
    for (int ks = 0; ks < 14; ++ks) {
        const v8bf av = *(const LAS v8bf*)(a + ks * 64);
        const v4s b0 = __builtin_amdgcn_ds_read_tr16_b64_v4i16((LAS v4s*)(bp + ks * 1024));
        const v4s b1 = __builtin_amdgcn_ds_read_tr16_b64_v4i16((LAS v4s*)(bp + ks * 1024 + 128));
        const v8s bb = __builtin_shufflevector(b0, b1, 0, 1, 2, 3, 4, 5, 6, 7);
        d = __builtin_amdgcn_mfma_f32_16x16x32_bf16(av, __builtin_bit_cast(v8bf, bb), d, 0, 0, 0);
    }
    return d[0];
}

#define STAMP(i) do { } while (0)
#define STAMP_FLUSH() do { } while (0)

__global__ void __launch_bounds__(NTHR, 2)
fused_decode_kernel(const u16* __restrict__ x, const u16* __restrict__ Kc, const u16* __restrict__ Vc, const u16* __restrict__ wq,
                    const u16* __restrict__ wo, const u16* __restrict__ w1, const u16* __restrict__ w2, u16* __restrict__ out,
                    unsigned char* __restrict__ ws) {
    __shared__ __attribute__((aligned(16))) char lds[L_END];
    LAS char* L = (LAS char*)lds;
    const int tid = threadIdx.x, lane = tid & 63, wave = __builtin_amdgcn_readfirstlane(tid >> 6);
    const int b = blockIdx.x, h = b & 7, s = b >> 3;
    const int j = h * 32 + s;
    const rsrc_t rws = __builtin_amdgcn_make_buffer_rsrc(ws, 0, WS_END, 0x00020000);
    unsigned tag = 0;
    unsigned wop[16];
    const int t4 = tid - 256;
    STAMP(0);

    if (wave < NSW) {
        const unsigned ring = __builtin_amdgcn_readfirstlane((unsigned)(size_t)L) + wave * 32768;
        const bool kdef = KDEF(h), vdef = VDEF(h);
        const int hf = lane >> 5, cp = lane & 31, g = lane >> 4, i = lane & 15;
        unsigned voffK[8], voffV[8];
#pragma unroll
        for (int p = 0; p < 8; ++p) { const int key = 2 * p + hf;
 voffK[p] = key * 512 + ((cp ^ (key & 15)) * 16); voffV[p] = key * 512 + ((cp ^ (4 * (key & 3))) * 16);
 }
        const char* Kh = (const char*)Kc + (size_t)h * SK * 512 + (size_t)s * (SK / NSPLIT * 512) + (size_t)wave * 8192;
        const char* Vh = (const char*)Vc + (size_t)h * SK * 512 + (size_t)s * (SK / NSPLIT * 512) + (size_t)wave * 8192;
        constexpr size_t TSTRIDE = (size_t)NSW * 8192;
        auto issueK = [&](int t, int slot) {
            const char* gb = Kh + (size_t)t * TSTRIDE;
            asm volatile("s_waitcnt lgkmcnt(0)" ::: "memory");
#pragma unroll
            for (int p = 0; p < 8; ++p) dmaK(ring + slot * 8192 + p * 1024, voffK[p], gb);
        };
        auto issueV = [&](int t, int slot) {
            const char* gb = Vh + (size_t)t * TSTRIDE;
            asm volatile("s_waitcnt lgkmcnt(0)" ::: "memory");
#pragma unroll
            for (int p = 0; p < 8; ++p) dmaV(ring + slot * 8192 + p * 1024, voffV[p], gb);
        };
        issueK(0, 0); issueV(0, 1); issueK(1, 2); issueV(1, 3);
        STAMP(1);
        WG_BARRIER();
        STAMP(2);
        tag = *(LAS unsigned*)(L + L_TAG);
        v8bf qf[8];
#pragma unroll
        for (int ks = 0; ks < 8; ++ks) qf[ks] = *(const LAS v8bf*)(L + L_Q + (i & 1) * 512 + ks * 64 + g * 16);
        LAS char* R = L + wave * 32768;
        unsigned ka[4], va[2];
#pragma unroll
        for (int k4 = 0; k4 < 4; ++k4) ka[k4] = i * 512 + ((k4 ^ (i >> 2)) * 64) + ((g ^ (i & 3)) * 16);
        const int q4 = (lane >> 2) & 3, p4 = lane & 3;
#pragma unroll
        for (int u1 = 0; u1 < 2; ++u1) va[u1] = q4 * 512 + (((8 * u1 + 2 * g + (p4 >> 1)) ^ (4 * q4)) * 16) + (p4 & 1) * 8;
        float m = -INFINITY, l = 0.f;
        v4f acc[4] = {{0.f, 0.f, 0.f, 0.f}, {0.f, 0.f, 0.f, 0.f}, {0.f, 0.f, 0.f, 0.f}, {0.f, 0.f, 0.f, 0.f}};
        v4s P;
#define SB() __builtin_amdgcn_sched_barrier(0)
#define PIECE_K(ISS, p) do { if (ISS) dmaK(ring + is * 8192 + (p) * 1024, voffK[p], gb); SB(); } while (0)
#define PIECE_V(ISS, p) do { if (ISS) dmaV(ring + is * 8192 + (p) * 1024, voffV[p], gb); SB(); } while (0)
#define PHASE_K(slot, ISS, tn) do { \
            constexpr int is = ((slot) + 3) & 3; const char* gb = Vh + (size_t)(tn) * TSTRIDE; \
            v8bf kf[8]; \
            _Pragma("unroll") for (int ks = 0; ks < 8; ++ks) kf[ks] = *(const LAS v8bf*)(R + (slot) * 8192 + (ks >> 2) * 256 + ka[ks & 3]); \
            SB(); PIECE_V(ISS, 0); \
            v4f sc = {0.f, 0.f, 0.f, 0.f}; \
            _Pragma("unroll") for (int ks = 0; ks < 4; ++ks) sc = __builtin_amdgcn_mfma_f32_16x16x32_bf16(kf[ks], qf[ks], sc, 0, 0, 0); \
            SB(); PIECE_V(ISS, 1); \
            _Pragma("unroll") for (int ks = 4; ks < 8; ++ks) sc = __builtin_amdgcn_mfma_f32_16x16x32_bf16(kf[ks], qf[ks], sc, 0, 0, 0); \
            SB(); PIECE_V(ISS, 2); \
            float d[4]; \
            _Pragma("unroll") for (int r = 0; r < 4; ++r) d[r] = rbf(sc[r]); \
            const float tm = max32(max16(fmaxf(fmaxf(d[0], d[1]), fmaxf(d[2], d[3])))); \
            const float mn = fmaxf(m, tm); \
            SB(); PIECE_V(ISS, 3); \
            const float alpha = ex2((m - mn) * SC2); \
            float p[4]; \
            _Pragma("unroll") for (int r = 0; r < 4; ++r) p[r] = ex2((d[r] - mn) * SC2); \
            m = mn; \
            SB(); PIECE_V(ISS, 4); \
            l = fmaf(l, alpha, (p[0] + p[1]) + (p[2] + p[3])); \
            const v2u pp = {pk2(p[0], p[1]), pk2(p[2], p[3])}; \
            P = __builtin_bit_cast(v4s, pp); \
            SB(); PIECE_V(ISS, 5); \
            const float a0 = dpp<0x00>(alpha), a1 = dpp<0x55>(alpha); \
            _Pragma("unroll") for (int u = 0; u < 4; ++u) { acc[u][0] *= a0; acc[u][1] *= a1; } \
            SB(); PIECE_V(ISS, 6); PIECE_V(ISS, 7); \
        } while (0)
#define PV_RD(kap, slot) _Pragma("unroll") for (int u = 0; u < 4; ++u) \
            vf[kap][u] = __builtin_amdgcn_ds_read_tr16_b64_v4i16((LAS v4s*)(R + (slot) * 8192 + (kap) * 2048 + (u >> 1) * 256 + va[u & 1]));
#define PV_MM(kap) _Pragma("unroll") for (int u = 0; u < 4; ++u) acc[u] = __builtin_amdgcn_mfma_f32_4x4x4bf16_1k(P, vf[kap][u], acc[u], 4, 4 * (kap), 0);
#define PHASE_V(slot, ISS, tn) do { \
            constexpr int is = ((slot) + 3) & 3; const char* gb = Kh + (size_t)(tn) * TSTRIDE; \
            v4s vf[4][4]; \
            PV_RD(0, slot) PV_RD(1, slot) SB(); PIECE_K(ISS, 0); \
            PV_RD(2, slot) PV_RD(3, slot) SB(); PIECE_K(ISS, 1); \
            PV_MM(0) SB(); PIECE_K(ISS, 2); \
            PV_MM(1) SB(); PIECE_K(ISS, 3); \
            PV_MM(2) SB(); PIECE_K(ISS, 4); \
            PV_MM(3) SB(); PIECE_K(ISS, 5); PIECE_K(ISS, 6); PIECE_K(ISS, 7); \
        } while (0)
        VMCNT(24); PHASE_K(0, 0, 0);
        VMCNT(16); PHASE_V(1, 1, 2);
        for (int t = 1; t < TILES - 3; t += 2) {
            VMCNT(16); PHASE_K(2, 1, t + 1);
            VMCNT(16); PHASE_V(3, 1, t + 2);
            VMCNT(16); PHASE_K(0, 1, t + 2);
            VMCNT(16); PHASE_V(1, 1, t + 3);
        }
        VMCNT(16); PHASE_K(2, 1, TILES - 2);
        VMCNT(16); PHASE_V(3, 1, TILES - 1);
        VMCNT(16); PHASE_K(0, 1, TILES - 1);
        STAMP(3);
        VMCNT(16); PHASE_V(1, 0, 0);
        VMCNT(8); PHASE_K(2, 0, 0);
        VMCNT(0); STAMP(4); PHASE_V(3, 0, 0);
        l = sum32(sum16(l));
        LAS float* part = (LAS float*)R;
#pragma unroll
        for (int u = 0; u < 4; ++u) { part[64 * u + lane] = acc[u][0]; part[256 + 64 * u + lane] = acc[u][1]; }
        if (lane < 2) { part[512 + 2 * lane] = m; part[513 + 2 * lane] = l; }
    } else {
        const int fs = h * 14 + s;
        v4u w2r[16];
        if (wave == 4) {
            v4u xv; unsigned ep;
            const u16* xp = x + (lane < 56 ? lane : 55) * 8;
            asm volatile("global_load_dwordx4 %0, %1, off" : "=v"(xv) : "v"(xp) : "memory");
            asm volatile("global_load_dword %0, %1, off sc1" : "=v"(ep) : "v"(ws + WS_EPOCH) : "memory");
            slice_dma(wq + j * 16, NQ, (unsigned)(size_t)L + L_W, lane);
            asm volatile("s_waitcnt vmcnt(15)" : "+v"(xv) :: "memory");
            if (lane >= 56) xv = v4u{0u, 0u, 0u, 0u};
            layernorm_to_lds(xv, L + L_XN, lane);
            STAMP(1);
            asm volatile("s_waitcnt vmcnt(0) lgkmcnt(0)" : "+v"(ep) :: "memory");
            STAMP(2);
            tag = ep + 1u;
            const float qv = slice_mfma(L, L_W, lane);
            const unsigned pr = pk2(qv, dpp<XOR1>(qv));
            const unsigned pr2 = __builtin_bit_cast(unsigned, dpp<XOR2>(__builtin_bit_cast(float, pr)));
            if (lane < 16 && (lane & 3) == 0) st16_sc1(rws, WS_QG + (unsigned)(j * 8 + (lane >> 1)) * 8, v4u{pr, tag, pr2, tag});
            STAMP(3);
            const unsigned off[2] = {WS_QG + h * 2048 + lane * 16, WS_QG + h * 2048 + 1024 + lane * 16};
            v4u v[2];
            if (QDELAY) __builtin_amdgcn_s_sleep(QDELAY);
            gather<2, QSLEEP>(rws, off, tag, v);
            *(LAS v2u*)(L + L_Q + lane * 8) = v2u{v[0].x, v[0].z};
            *(LAS v2u*)(L + L_Q + 512 + lane * 8) = v2u{v[1].x, v[1].z};
            if (lane == 0) *(LAS unsigned*)(L + L_TAG) = tag;
            STAMP(4);
        } else {
        }
        WG_BARRIER();
        tag = *(LAS unsigned*)(L + L_TAG);
        if (wave == 5 && b < NFFN) {
            slice_dma(w1 + fs * 16, DFF, (unsigned)(size_t)L + L_W1, lane);
            const int lc = lane < 56 ? lane : 55;
#pragma unroll
            for (int r = 0; r < 16; ++r) w2r[r] = wload((const v4u*)(w2 + (size_t)(fs * 16 + r) * D + lc * 8));
            VMCNT(0);
            const float hv16 = fmaxf(rbf(slice_mfma(L, L_W1, lane)), 0.f);
            float o[8];
#pragma unroll
            for (int k = 0; k < 8; ++k) o[k] = 0.f;
#pragma unroll
            for (int r = 0; r < 16; ++r) {
                const float hv = __builtin_bit_cast(float, __builtin_amdgcn_readlane(__builtin_bit_cast(int, hv16), r));
#pragma unroll
                for (int k = 0; k < 4; ++k) { o[2 * k] = fmaf(hv, bflo(w2r[r][k]), o[2 * k]); o[2 * k + 1] = fmaf(hv, bfhi(w2r[r][k]), o[2 * k + 1]); }
            }
            if (lane < 56) {
                const unsigned off = WS_FG + (unsigned)(lane * NFFN + fs) * 64;
#pragma unroll
                for (int k = 0; k < 4; ++k) st16_sc1(rws, off + 16 * k, v4u{__float_as_uint(o[2 * k]), tag, __float_as_uint(o[2 * k + 1]), tag});
            }
        }
        if (!WEARLY || wave == 4) {
            const int tc = t4 < 224 ? t4 : 223;
#pragma unroll
            for (int r = 0; r < 16; ++r) wop[r] = wload((const unsigned*)(wo + (size_t)(j * 16 + r) * D + 2 * tc));
        }
        if (wave == 6 && b < NRED) {
            for (int z = 0; z < FSLEEP; ++z) __builtin_amdgcn_s_sleep(127);
            unsigned off[7];
#pragma unroll
            for (int k = 0; k < 7; ++k) off[k] = WS_FG + (unsigned)b * NFFN * 64 + k * 1024 + lane * 16;
            v4u v[7];
            gather<7, 64>(rws, off, tag, v);
            float s0 = 0.f, s1 = 0.f;
#pragma unroll
            for (int k = 0; k < 7; ++k) { s0 += __uint_as_float(v[k].x); s1 += __uint_as_float(v[k].z); }
            s0 += dpp<ROR4>(s0); s0 += dpp<ROR8>(s0); s0 = sum32(sum16(s0));
            s1 += dpp<ROR4>(s1); s1 += dpp<ROR8>(s1); s1 = sum32(sum16(s1));
            if (lane < 4) {
                LAS float* fx = (LAS float*)(L + L_FX);
                fx[2 * lane] = rbf(s0); fx[2 * lane + 1] = rbf(s1);
                const unsigned xw = *(const GAS unsigned*)(x + b * 8 + 2 * lane);
                fx[8 + 2 * lane] = bflo(xw); fx[8 + 2 * lane + 1] = bfhi(xw);
            }
        }
#pragma unroll
        for (int r = 0; r < 8; ++r) {
            const unsigned a = wop[2 * r], c = wop[2 * r + 1];
            wop[2 * r] = __builtin_amdgcn_perm(c, a, 0x05040100u);
            wop[2 * r + 1] = __builtin_amdgcn_perm(c, a, 0x07060302u);
        }
        STAMP(6);
    }
    WG_BARRIER();
    if (wave < 4) STAMP(5);
    {
        const int hh = tid >> 8, d = tid & 255;
        float mw[NSW], lw[NSW], aw[NSW];
#pragma unroll
        for (int w = 0; w < NSW; ++w) {
            const LAS float* part = (const LAS float*)(L + w * 32768);
            mw[w] = part[512 + 2 * hh]; lw[w] = part[513 + 2 * hh]; aw[w] = part[hh * 256 + d];
        }
        const float M = fmaxf(fmaxf(mw[0], mw[1]), fmaxf(mw[2], mw[3]));
        float A = 0.f, Ls = 0.f;
#pragma unroll
        for (int w = 0; w < NSW; ++w) { const float f = ex2((mw[w] - M) * SC2); A = fmaf(f, aw[w], A); Ls = fmaf(f, lw[w], Ls); }
        const int hq = 2 * h + hh;
        st8_sc1(rws, WS_AG + (unsigned)(((hq * 16 + (d >> 4)) * 32 + s) * 16 + (d & 15)) * 8, v2u{__float_as_uint(A), tag});
        if (d == 0) st16_sc1(rws, WS_ML + (unsigned)(hq * 32 + s) * 16, v4u{__float_as_uint(M), tag, __float_as_uint(Ls), tag});
    }
    if (wave < 4) STAMP(6);
    if (wave == 4) {
        unsigned off[8];
#pragma unroll
        for (int k = 0; k < 4; ++k) { off[k] = WS_AG + (unsigned)j * 4096 + k * 1024 + lane * 16; off[4 + k] = WS_ML + (unsigned)((j >> 4) * 32 + (lane >> 3) + 8 * k) * 16; }
        v4u v[8];
        {
            const unsigned offm[1] = {WS_ML + (unsigned)((j >> 4) * 32 + (lane & 31)) * 16};
            v4u vm[1];
            gather<1, MLFIRST>(rws, offm, tag, vm);
        }
        TAILG1(rws, off, tag, v);
        float M = fmaxf(fmaxf(__uint_as_float(v[4].x), __uint_as_float(v[5].x)), fmaxf(__uint_as_float(v[6].x), __uint_as_float(v[7].x)));
        M = max32(max16(fmaxf(M, dpp<ROR8>(M))));
        float Ls = 0.f, o0 = 0.f, o1 = 0.f;
#pragma unroll
        for (int k = 0; k < 4; ++k) {
            const float f = ex2((__uint_as_float(v[4 + k].x) - M) * SC2);
            Ls = fmaf(f, __uint_as_float(v[4 + k].z), Ls); o0 = fmaf(f, __uint_as_float(v[k].x), o0); o1 = fmaf(f, __uint_as_float(v[k].z), o1);
        }
        Ls += dpp<ROR8>(Ls); o0 += dpp<ROR8>(o0); o1 += dpp<ROR8>(o1);
        Ls = sum32(sum16(Ls)); o0 = sum32(sum16(o0)); o1 = sum32(sum16(o1));
        if (lane < 8) *(LAS unsigned*)(L + L_O + lane * 4) = pk2(o0 / Ls, o1 / Ls);
        STAMP(7);
    }
    WG_BARRIER();
    if (wave >= NSW && t4 < 224) {
        const v4u oa = *(const LAS v4u*)(L + L_O), ob = *(const LAS v4u*)(L + L_O + 16);
        float c0 = 0.f, c1 = 0.f;
#pragma unroll
        for (int r = 0; r < 4; ++r) { c0 = dot2(wop[2 * r], oa[r], c0); c1 = dot2(wop[2 * r + 1], oa[r], c1); }
#pragma unroll
        for (int r = 0; r < 4; ++r) { c0 = dot2(wop[8 + 2 * r], ob[r], c0); c1 = dot2(wop[9 + 2 * r], ob[r], c1); }
        st16_sc1(rws, WS_OG + (unsigned)((t4 >> 2) * 256 + j) * 64 + (t4 & 3) * 16, v4u{__float_as_uint(c0), tag, __float_as_uint(c1), tag});
    }
    if (b < NRED) {
        const unsigned off[2] = {WS_OG + (unsigned)b * 16384 + wave * 2048 + lane * 16, WS_OG + (unsigned)b * 16384 + wave * 2048 + 1024 + lane * 16};
        v4u v[2];
        TAILG2(rws, off, tag, v);
        float s0 = __uint_as_float(v[0].x) + __uint_as_float(v[1].x), s1 = __uint_as_float(v[0].z) + __uint_as_float(v[1].z);
        s0 += dpp<ROR4>(s0); s0 += dpp<ROR8>(s0); s0 = sum32(sum16(s0));
        s1 += dpp<ROR4>(s1); s1 += dpp<ROR8>(s1); s1 = sum32(sum16(s1));
        if (lane < 4) { LAS float* red = (LAS float*)(L + L_RED) + wave * 8; red[2 * lane] = s0; red[2 * lane + 1] = s1; }
        WG_BARRIER();
        if (tid < 8) {
            const LAS float* red = (const LAS float*)(L + L_RED);
            float a = 0.f;
#pragma unroll
            for (int w = 0; w < 8; ++w) a += red[w * 8 + tid];
            const LAS float* fx = (const LAS float*)(L + L_FX);
            const float y = rbf(fx[8 + tid] + rbf(a)) + fx[tid];
            out[b * 8 + tid] = __builtin_bit_cast(u16, (__bf16)y);
        }
        if (b == 0 && tid == 0) __hip_atomic_store((GAS unsigned*)(ws + WS_EPOCH), tag, __ATOMIC_RELAXED, __HIP_MEMORY_SCOPE_AGENT);
        if (wave == 7) STAMP(7);
    }
    STAMP_FLUSH();
}

}

extern "C" void kernel_launch(void* const* d_in, const int* in_sizes, int n_in, void* d_out, int out_size, void* d_ws, size_t ws_size,
                              hipStream_t stream) {
    (void)in_sizes; (void)n_in; (void)out_size; (void)ws_size;
    fprintf(stderr, "PTRS:"); for (int i = 0; i < n_in; ++i) fprintf(stderr, " in%d=%p", i, d_in[i]); fprintf(stderr, " out=%p ws=%p\n", d_out, d_ws);
    printf("PTRS:"); for (int i = 0; i < n_in; ++i) printf(" in%d=%p", i, d_in[i]); printf(" out=%p ws=%p\n", d_out, d_ws); fflush(stdout);
    hipLaunchKernelGGL(fused_decode_kernel, dim3(NWG), dim3(NTHR), 0, stream,
                       (const u16*)d_in[0], (const u16*)d_in[1], (const u16*)d_in[2], (const u16*)d_in[3], (const u16*)d_in[4],
                       (const u16*)d_in[6], (const u16*)d_in[7], (u16*)d_out, (unsigned char*)d_ws);
}
```

```cpp
#include <hip/hip_runtime.h>
#include <hip/hip_bf16.h>
#include <stdint.h>

#define GAS __attribute__((address_space(1)))
#define LAS __attribute__((address_space(3)))

#define NFLY 6
#define BOOST_X 2
#define AD_UA 8
#define AD_UB 14
#define AD_MARGIN 28
#define AD_AHEAD 15
#define AD_SCALE 100
#define AD_WAIT 5
#define QDELAY 40
#define QSLEEP 4
#define HDELAY 60
#define FSLEEP 3
#define H1DELAY 22
#define MLSLEEP 4
#define H2DELAY 40
#define TM0 0
#define TM3 12
#define TM4 0

namespace {

typedef unsigned short u16;
typedef unsigned v4u __attribute__((ext_vector_type(4)));
typedef unsigned v2u __attribute__((ext_vector_type(2)));
typedef float v4f __attribute__((ext_vector_type(4)));
typedef short v4s __attribute__((ext_vector_type(4)));
typedef short v8s __attribute__((ext_vector_type(8)));
typedef __bf16 v8bf __attribute__((ext_vector_type(8)));
typedef __bf16 bf16x2 __attribute__((ext_vector_type(2)));

constexpr int D = 448, SK = 32768, DFF = 1792, NQ = 4096;
constexpr int NWG = 256, NTHR = 512;
constexpr int NSPLIT = 32;
constexpr int NSW = 4;
constexpr int TILES = SK / NSPLIT / NSW / 16;
constexpr int NFFN = DFF / 16;
constexpr int NRED = D / 8;
constexpr int NCON = 128;
constexpr float SC2 = 0.0625f * 1.4426950408889634f;
constexpr unsigned SPIN_CAP = 1u << 18;
static_assert(TILES == 16, "the phase list below is written out for 16 tiles");

constexpr unsigned WS_EPOCH = 0;
constexpr unsigned WS_QG = 4096;
constexpr unsigned WS_ML = WS_QG + 8 * 4 * 512 * 8;
constexpr unsigned WS_AG = WS_ML + 16 * 32 * 16;
constexpr unsigned WS_OG = WS_AG + 256 * 4096;
constexpr unsigned WS_FG = WS_OG + NRED * NCON * 64;
constexpr unsigned WS_HG = WS_FG + NRED * NFFN * 64;
constexpr unsigned WS_PG = WS_HG + 28 * 4 * 64 * 8;
constexpr unsigned WS_END = WS_PG + 256 * 16;

constexpr unsigned L_Q = 131072;
constexpr unsigned L_XN = L_Q + 1024;
constexpr unsigned L_WA = L_XN + 896;
constexpr unsigned L_WB = L_WA + 14336;
constexpr unsigned L_O = L_WB + 14336;
constexpr unsigned L_RED = L_O + 64;
constexpr unsigned L_FX = L_RED + 256;
constexpr unsigned L_TAG = L_FX + 64;
constexpr unsigned L_SCHED = L_TAG + 16;
constexpr unsigned L_END = L_SCHED + 16;

__device__ __forceinline__ float bflo(unsigned w) { return __uint_as_float(w << 16); }
__device__ __forceinline__ float bfhi(unsigned w) { return __uint_as_float(w & 0xffff0000u); }
__device__ __forceinline__ float rbf(float f) { return (float)(__bf16)f; }
__device__ __forceinline__ unsigned pk2(float a, float b) { bf16x2 v = {(__bf16)a, (__bf16)b}; return __builtin_bit_cast(unsigned, v); }
template <int CTRL> __device__ __forceinline__ float dpp(float x) {
    return __builtin_bit_cast(float, __builtin_amdgcn_mov_dpp(__builtin_bit_cast(int, x), CTRL, 0xf, 0xf, true));
}
constexpr int XOR1 = 0xB1, XOR2 = 0x4E, ROR4 = 0x124, ROR8 = 0x128;
__device__ __forceinline__ float sum16(float x) {
    auto s = __builtin_amdgcn_permlane16_swap(__float_as_uint(x), __float_as_uint(x), false, false);
    return __uint_as_float(s[0]) + __uint_as_float(s[1]);
}
__device__ __forceinline__ float sum32(float x) {
    auto s = __builtin_amdgcn_permlane32_swap(__float_as_uint(x), __float_as_uint(x), false, false);
    return __uint_as_float(s[0]) + __uint_as_float(s[1]);
}
__device__ __forceinline__ float max16(float x) {
    auto s = __builtin_amdgcn_permlane16_swap(__float_as_uint(x), __float_as_uint(x), false, false);
    return fmaxf(__uint_as_float(s[0]), __uint_as_float(s[1]));
}
__device__ __forceinline__ float max32(float x) {
    auto s = __builtin_amdgcn_permlane32_swap(__float_as_uint(x), __float_as_uint(x), false, false);
    return fmaxf(__uint_as_float(s[0]), __uint_as_float(s[1]));
}
__device__ __forceinline__ float wave_sum(float x) {
    x += dpp<XOR1>(x); x += dpp<XOR2>(x); x += dpp<ROR4>(x); x += dpp<ROR8>(x);
    return sum32(sum16(x));
}
__device__ __forceinline__ float ex2(float x) { return __builtin_amdgcn_exp2f(x); }

#define WG_BARRIER() do { asm volatile("s_waitcnt lgkmcnt(0)" ::: "memory"); __builtin_amdgcn_s_barrier(); asm volatile("" ::: "memory"); } while (0)
#define VMCNT(n) asm volatile("s_waitcnt vmcnt(%0)" :: "i"(n) : "memory")
#define SB() __builtin_amdgcn_sched_barrier(0)

__device__ __forceinline__ void dma(unsigned ldsaddr, unsigned voff, const void* gbase) {
    asm volatile("s_mov_b32 m0, %0\n\ts_nop 0\n\tglobal_load_lds_dwordx4 %1, %2 nt" :: "s"(ldsaddr), "v"(voff), "s"(gbase) : "memory");
}
template <int N> __device__ __forceinline__ void dma_win(unsigned ldsaddr, unsigned voff, const void* gbase) {
    asm volatile("s_waitcnt vmcnt(%3)\n\ts_mov_b32 m0, %0\n\ts_nop 0\n\tglobal_load_lds_dwordx4 %1, %2 nt" :: "s"(ldsaddr), "v"(voff), "s"(gbase), "i"(N - 1) : "memory");
}
template <int N, int X> __device__ __forceinline__ void dma_win3(unsigned ldsaddr, unsigned voff, const void* gbase, unsigned level) {
    asm volatile("s_cmp_gt_u32 %4, 1\n\ts_cbranch_scc1 2f\n\ts_cmp_eq_u32 %4, 1\n\ts_cbranch_scc1 1f\n\ts_waitcnt vmcnt(%6)\n1:\n\ts_waitcnt vmcnt(%3)\n2:\n\ts_waitcnt vmcnt(%5)\n\t"
                 "s_mov_b32 m0, %0\n\ts_nop 0\n\tglobal_load_lds_dwordx4 %1, %2 nt"
                 :: "s"(ldsaddr), "v"(voff), "s"(gbase), "i"(N - 1), "s"(level), "i"(N + X - 1), "i"(N - 2) : "memory", "scc");
}

typedef __amdgpu_buffer_rsrc_t rsrc_t;
__device__ __forceinline__ v4u ld16_sc1(rsrc_t r, unsigned off) { return __builtin_amdgcn_raw_buffer_load_b128(r, off, 0, 16); }
__device__ __forceinline__ void st16_sc1(rsrc_t r, unsigned off, v4u v) { __builtin_amdgcn_raw_buffer_store_b128(v, r, off, 0, 16); }

__device__ __forceinline__ void publish_pair(rsrc_t r, unsigned off, float v, unsigned tag, int lane) {
    const float nb = dpp<XOR1>(v);
    if (!(lane & 1)) st16_sc1(r, off, v4u{__float_as_uint(v), tag, __float_as_uint(nb), tag});
}
template <int N, int SLEEP> __device__ __forceinline__ void gather(rsrc_t r, const unsigned (&off)[N], unsigned tag, v4u (&v)[N]) {
    for (unsigned spin = 0; spin < SPIN_CAP; ++spin) {
        asm volatile("" ::: "memory");
        bool ok = true;
#pragma unroll
        for (int k = 0; k < N; ++k) v[k] = ld16_sc1(r, off[k]);
#pragma unroll
        for (int k = 0; k < N; ++k) ok &= (v[k].y == tag) & (v[k].w == tag);
        if (__all(ok)) break;
        __builtin_amdgcn_s_sleep(SLEEP);
    }
}

__device__ __forceinline__ void layernorm_to_lds(v4u xv, LAS char* xn, int lane) {
    float f[8];
#pragma unroll
    for (int k = 0; k < 4; ++k) { f[2 * k] = bflo(xv[k]); f[2 * k + 1] = bfhi(xv[k]); }
    float s = 0.f;
#pragma unroll
    for (int k = 0; k < 8; ++k) s += f[k];
    const float mean = wave_sum(s) * (1.0f / D);
    float v = 0.f;
#pragma unroll
    for (int k = 0; k < 8; ++k) { const float d = f[k] - mean; v = fmaf(d, d, v); }
    if (lane >= 56) v = 0.f;
    const float var = wave_sum(v) * (1.0f / D);
    const float mb = rbf(mean), den = rbf(sqrtf(rbf(rbf(var) + 1e-5f)));
    v4u o;
#pragma unroll
    for (int k = 0; k < 4; ++k) o[k] = pk2(rbf(f[2 * k] - mb) / den, rbf(f[2 * k + 1] - mb) / den);
    if (lane < 56) *(LAS v4u*)(xn + lane * 16) = o;
}

__device__ __forceinline__ float pick4(int g, float a, float b, float c, float d) { return g == 0 ? a : g == 1 ? b : g == 2 ? c : d; }
__device__ __forceinline__ void block_dma(const u16* w, int ld, unsigned ldsaddr, int lane) {
    const unsigned voff = (unsigned)(lane >> 3) * (ld * 2) + (lane & 7) * 16;
#pragma unroll
    for (int i = 0; i < 14; ++i) dma(ldsaddr + i * 1024, voff, (const char*)w + (size_t)i * 8 * ld * 2);
}
__device__ __forceinline__ float block_mfma(LAS char* L, unsigned wofs, int rb, int lane) {
    const int g = lane >> 4, i = lane & 15;
    v4f d[4] = {{0.f, 0.f, 0.f, 0.f}, {0.f, 0.f, 0.f, 0.f}, {0.f, 0.f, 0.f, 0.f}, {0.f, 0.f, 0.f, 0.f}};
    LAS char* ap = L + L_XN + rb * 224 + g * 8;
    LAS char* bp = L + wofs + (4 * g + (i >> 2)) * 128 + (i & 3) * 8;
#pragma unroll
    for (int ks = 0; ks < 7; ++ks) {
        const v4s av = *(const LAS v4s*)(ap + ks * 32);
#pragma unroll
        for (int t = 0; t < 4; ++t) {
            const v4s bv = __builtin_amdgcn_ds_read_tr16_b64_v4i16((LAS v4s*)(bp + ks * 2048 + t * 32));
            d[t] = __builtin_amdgcn_mfma_f32_16x16x16bf16_1k(av, bv, d[t], 0, 0, 0);
        }
    }
    return pick4(g, d[0][0], d[1][0], d[2][0], d[3][0]);
}

#define STAMP(i) do { } while (0)
#define STAMP_FLUSH() do { } while (0)

__global__ void __launch_bounds__(NTHR, 2)
fused_decode_kernel(const u16* __restrict__ x, const u16* __restrict__ Kc, const u16* __restrict__ Vc, const u16* __restrict__ wq,
                    const u16* __restrict__ wo, const u16* __restrict__ w1, const u16* __restrict__ w2, u16* __restrict__ out,
                    unsigned char* __restrict__ ws) {
    __shared__ __attribute__((aligned(16))) char lds[L_END];
    LAS char* L = (LAS char*)lds;
    STAMP(0);
    asm volatile("" :: "s"(x), "s"(Kc), "s"(Vc), "s"(wq), "s"(wo), "s"(w1), "s"(w2), "s"(out), "s"(ws));
    const int tid = threadIdx.x, lane = tid & 63, wave = __builtin_amdgcn_readfirstlane(tid >> 6);
    const int b = blockIdx.x, h = b & 7, s = b >> 3;
    const int g = lane >> 4, i16 = lane & 15;
    const unsigned Lb = __builtin_amdgcn_readfirstlane((unsigned)(size_t)L);
    const rsrc_t rws = __builtin_amdgcn_make_buffer_rsrc(ws, 0, WS_END, 0x00020000);
    const bool is_ffn = s < 14, is_con = s >= 16, is_red = b < NRED;
    const int j2 = h * 16 + (s - 16);
    unsigned tag = 0;

    if (wave < NSW) {
        const unsigned ring = Lb + wave * 32768;
        const int hf = lane >> 5, cp = lane & 31;
        unsigned voffK[8], voffV[8];
#pragma unroll
        for (int p = 0; p < 8; ++p) { const int key = 2 * p + hf; voffK[p] = key * 512 + ((cp ^ (key & 15)) * 16); voffV[p] = key * 512 + ((cp ^ (4 * (key & 3))) * 16); }
        const char* Kh = (const char*)Kc + (size_t)h * SK * 512 + (size_t)s * (SK / NSPLIT * 512) + (size_t)wave * 8192;
        const char* Vh = (const char*)Vc + (size_t)h * SK * 512 + (size_t)s * (SK / NSPLIT * 512) + (size_t)wave * 8192;
        constexpr size_t TSTRIDE = (size_t)NSW * 8192;
        const int tmask = ((s & 1) ? TM0 : 0) ^ ((s & 8) ? TM3 : 0) ^ ((s & 16) ? TM4 : 0);
#pragma unroll
        for (int t = 0; t < 4; ++t)
#pragma unroll
            for (int p = 0; p < 8; ++p) dma_win<NFLY - 1>(ring + t * 8192 + p * 1024, voffK[p], Kh + (size_t)(t ^ tmask) * TSTRIDE);
        STAMP(1);
        WG_BARRIER();
        STAMP(2);
        tag = *(LAS unsigned*)(L + L_TAG);
        v8bf qf[8];
#pragma unroll
        for (int ks = 0; ks < 8; ++ks) qf[ks] = *(const LAS v8bf*)(L + L_Q + (i16 & 1) * 512 + ks * 64 + g * 16);
        LAS char* R = L + wave * 32768;
        unsigned ka[4], va[2];
#pragma unroll
        for (int k4 = 0; k4 < 4; ++k4) ka[k4] = i16 * 512 + ((k4 ^ (i16 >> 2)) * 64) + ((g ^ (i16 & 3)) * 16);
        const int q4 = (lane >> 2) & 3, p4 = lane & 3;
#pragma unroll
        for (int u1 = 0; u1 < 2; ++u1) va[u1] = q4 * 512 + (((8 * u1 + 2 * g + (p4 >> 1)) ^ (4 * q4)) * 16) + (p4 & 1) * 8;
#define TS(u) do { } while (0)
        v2u sc[TILES];
        float mx = -INFINITY, m = 0.f, l = 0.f;
        v4f acc[4] = {{0.f, 0.f, 0.f, 0.f}, {0.f, 0.f, 0.f, 0.f}, {0.f, 0.f, 0.f, 0.f}, {0.f, 0.f, 0.f, 0.f}};
        unsigned level = 0, t_a = 0;

#define PIECE(u, p) do { if ((u) >= 1 && (u) <= 28) { \
            if ((u) + 3 < TILES) dma_win3<NFLY, BOOST_X>(ring + (((u) + 3) & 3) * 8192 + (p) * 1024, voffK[p], gb, level); \
            else dma_win3<NFLY, BOOST_X>(ring + (((u) + 3) & 3) * 8192 + (p) * 1024, voffV[p], gb, level); } SB(); } while (0)
#define PACE(u) do { TS(u); if ((u) >= 1 && (u) <= 28) { \
            if ((u) == AD_UA || (u) >= AD_UB) { const unsigned now_ = (unsigned)__builtin_amdgcn_s_memrealtime(); \
                if ((u) == AD_UA) t_a = now_; \
                if ((u) == AD_UB && wave == 0 && lane == 0) st16_sc1(rws, WS_PG + b * 16, v4u{t_a, tag, now_, tag}); \
                if ((u) > AD_UB) { const v4u sc_ = *(const LAS v4u*)(L + L_SCHED); \
                    const int lag_ = (int)(now_ - (sc_.x + ((((u) - AD_UB) * sc_.y) >> 4))); \
                    level = __builtin_amdgcn_readfirstlane(sc_.z != tag ? 0u : lag_ > 0 ? 2u : lag_ < -AD_AHEAD ? 0u : 1u); } } } } while (0)
#define STEP_BASE(u) const char* gb = ((u) + 3 < TILES ? Kh + (size_t)(((u) + 3) ^ tmask) * TSTRIDE : Vh + (size_t)((((u) + 3 - TILES) & 15) ^ tmask) * TSTRIDE)
#define LANDED(u) VMCNT((u) == 0 ? 24 : (u) <= 29 ? 16 : (u) == 30 ? 8 : 0)
#define SOFTMAX(t) do { \
            const float p0 = ex2((bflo(sc[t].x) - m) * SC2), p1 = ex2((bfhi(sc[t].x) - m) * SC2), p2 = ex2((bflo(sc[t].y) - m) * SC2), p3 = ex2((bfhi(sc[t].y) - m) * SC2); \
            l += (p0 + p1) + (p2 + p3); \
            sc[t] = v2u{pk2(p0, p1), pk2(p2, p3)}; } while (0)
#define PHASE_K(u) do { \
            PACE(u); LANDED(u); STEP_BASE(u); \
            v8bf kf[8]; \
            _Pragma("unroll") for (int ks = 0; ks < 8; ++ks) kf[ks] = *(const LAS v8bf*)(R + ((u) & 3) * 8192 + (ks >> 2) * 256 + ka[ks & 3]); \
            SB(); PIECE(u, 0); \
            v4f d = {0.f, 0.f, 0.f, 0.f}; \
            _Pragma("unroll") for (int ks = 0; ks < 4; ++ks) d = __builtin_amdgcn_mfma_f32_16x16x32_bf16(kf[ks], qf[ks], d, 0, 0, 0); \
            SB(); PIECE(u, 1); \
            _Pragma("unroll") for (int ks = 4; ks < 8; ++ks) d = __builtin_amdgcn_mfma_f32_16x16x32_bf16(kf[ks], qf[ks], d, 0, 0, 0); \
            SB(); PIECE(u, 2); \
            sc[u] = v2u{pk2(d[0], d[1]), pk2(d[2], d[3])}; \
            mx = fmaxf(fmaxf(mx, fmaxf(d[0], d[1])), fmaxf(d[2], d[3])); \
            SB(); PIECE(u, 3); \
            if ((u) == TILES - 1) { m = max32(max16(rbf(mx))); SB(); PIECE(u, 4); SOFTMAX(0); SB(); } else PIECE(u, 4); \
            PIECE(u, 5); PIECE(u, 6); PIECE(u, 7); \
        } while (0)
#define PV_RD(kap, u) _Pragma("unroll") for (int c = 0; c < 4; ++c) \
            vf[kap][c] = __builtin_amdgcn_ds_read_tr16_b64_v4i16((LAS v4s*)(R + ((u) & 3) * 8192 + (kap) * 2048 + (c >> 1) * 256 + va[c & 1]));
#define PV_MM(kap) _Pragma("unroll") for (int c = 0; c < 4; ++c) acc[c] = __builtin_amdgcn_mfma_f32_4x4x4bf16_1k(P, vf[kap][c], acc[c], 4, 4 * (kap), 0);
#define PHASE_V(u) do { \
            PACE(u); LANDED(u); STEP_BASE(u); \
            if ((u) == 31) STAMP(4); \
            const v4s P = __builtin_bit_cast(v4s, sc[(u) - TILES]); \
            v4s vf[4][4]; \
            PV_RD(0, u) PV_RD(1, u) SB(); PIECE(u, 0); \
            PV_RD(2, u) PV_RD(3, u) SB(); PIECE(u, 1); \
            PV_MM(0) SB(); PIECE(u, 2); \
            PV_MM(1) SB(); PIECE(u, 3); \
            PV_MM(2) SB(); PIECE(u, 4); \
            PV_MM(3) SB(); PIECE(u, 5); \
            if ((u) < 31) { SOFTMAX(((u) - TILES + 1) & 15); SB(); } \
            PIECE(u, 6); PIECE(u, 7); \
            if ((u) == 28) STAMP(3); \
        } while (0)
        PHASE_K(0); PHASE_K(1); PHASE_K(2); PHASE_K(3); PHASE_K(4); PHASE_K(5); PHASE_K(6); PHASE_K(7);
        PHASE_K(8); PHASE_K(9); PHASE_K(10); PHASE_K(11); PHASE_K(12); PHASE_K(13); PHASE_K(14); PHASE_K(15);
        PHASE_V(16); PHASE_V(17); PHASE_V(18); PHASE_V(19); PHASE_V(20); PHASE_V(21); PHASE_V(22); PHASE_V(23);
        PHASE_V(24); PHASE_V(25); PHASE_V(26); PHASE_V(27); PHASE_V(28); PHASE_V(29); PHASE_V(30); PHASE_V(31);
        l = sum32(sum16(l));
        LAS float* part = (LAS float*)R;
#pragma unroll
        for (int u = 0; u < 4; ++u) { part[64 * u + lane] = acc[u][0]; part[256 + 64 * u + lane] = acc[u][1]; }
        if (lane < 2) { part[512 + 2 * lane] = m; part[513 + 2 * lane] = l; }
    } else {
        if (wave == 4) {
            const int rb = s & 3, cb = s >> 2;
            v4u xv; unsigned ep;
            const u16* xp = x + (lane < 56 ? lane : 55) * 8;
            asm volatile("global_load_dwordx4 %0, %1, off" : "=v"(xv) : "v"(xp) : "memory");
            asm volatile("global_load_dword %0, %1, off sc1" : "=v"(ep) : "v"(ws + WS_EPOCH) : "memory");
            block_dma(wq + (size_t)rb * 112 * NQ + h * 512 + cb * 64, NQ, Lb + L_WA, lane);
            asm volatile("s_waitcnt vmcnt(15)" : "+v"(xv) :: "memory");
            if (lane >= 56) xv = v4u{0u, 0u, 0u, 0u};
            layernorm_to_lds(xv, L + L_XN, lane);
            STAMP(1);
            asm volatile("s_waitcnt vmcnt(0) lgkmcnt(0)" : "+v"(ep) :: "memory");
            STAMP(2);
            tag = ep + 1u;
            publish_pair(rws, WS_QG + (unsigned)((h * 4 + rb) * 512 + cb * 64 + lane) * 8, block_mfma(L, L_WA, rb, lane), tag, lane);
            STAMP(3);
            unsigned off[16];
#pragma unroll
            for (int k = 0; k < 4; ++k)
#pragma unroll
                for (int r = 0; r < 4; ++r) off[4 * k + r] = WS_QG + (unsigned)((h * 4 + r) * 512 + 128 * k + 2 * lane) * 8;
            v4u v[16];
            __builtin_amdgcn_s_sleep(QDELAY);
            gather<16, QSLEEP>(rws, off, tag, v);
#pragma unroll
            for (int k = 0; k < 4; ++k) {
                const float q0 = (__uint_as_float(v[4 * k].x) + __uint_as_float(v[4 * k + 1].x)) + (__uint_as_float(v[4 * k + 2].x) + __uint_as_float(v[4 * k + 3].x));
                const float q1 = (__uint_as_float(v[4 * k].z) + __uint_as_float(v[4 * k + 1].z)) + (__uint_as_float(v[4 * k + 2].z) + __uint_as_float(v[4 * k + 3].z));
                *(LAS unsigned*)(L + L_Q + (64 * k + lane) * 4) = pk2(q0, q1);
            }
            if (lane == 0) *(LAS unsigned*)(L + L_TAG) = tag;
            STAMP(4);
        }
        WG_BARRIER();
        tag = *(LAS unsigned*)(L + L_TAG);
        if (wave == 5 && is_ffn) {
            const int fs = h * 14 + s, cb = fs >> 2, rb = fs & 3;
            block_dma(w1 + (size_t)rb * 112 * DFF + cb * 64, DFF, Lb + L_WB, lane);
#pragma unroll
            for (int p = 0; p < 14; ++p) dma(Lb + L_WA + p * 1024, lane * 16, (const char*)(w2 + (size_t)fs * 16 * D) + p * 1024);
            VMCNT(14);
            publish_pair(rws, WS_HG + (unsigned)((cb * 4 + rb) * 64 + lane) * 8, block_mfma(L, L_WB, rb, lane), tag, lane);
            const unsigned offh[1] = {WS_HG + (unsigned)((cb * 4 + ((lane >> 3) & 3)) * 64 + 16 * rb + 2 * (lane & 7)) * 8};
            v4u vh[1];
            __builtin_amdgcn_s_sleep(HDELAY);
            gather<1, 16>(rws, offh, tag, vh);
            float h0 = __uint_as_float(vh[0].x), h1 = __uint_as_float(vh[0].z);
            h0 += dpp<ROR8>(h0); h1 += dpp<ROR8>(h1); h0 = sum16(h0); h1 = sum16(h1);
            if (lane < 8) *(LAS unsigned*)(L + L_O + lane * 4) = pk2(fmaxf(rbf(h0), 0.f), fmaxf(rbf(h1), 0.f));
            asm volatile("s_waitcnt vmcnt(0) lgkmcnt(0)" ::: "memory");
            const v4s ha = *(const LAS v4s*)(L + L_O + g * 8);
            LAS char* bp = L + L_WA + (4 * g + (i16 >> 2)) * (D * 2) + (i16 & 3) * 8;
#pragma unroll
            for (int rr = 0; rr < 7; ++rr) {
                float dd[4];
#pragma unroll
                for (int t = 0; t < 4; ++t) {
                    const v4s bv = __builtin_amdgcn_ds_read_tr16_b64_v4i16((LAS v4s*)(bp + (4 * rr + t) * 32));
                    const v4f z = {0.f, 0.f, 0.f, 0.f};
                    dd[t] = __builtin_amdgcn_mfma_f32_16x16x16bf16_1k(ha, bv, z, 0, 0, 0)[0];
                }
                const int c = 64 * rr + lane;
                publish_pair(rws, WS_FG + (unsigned)((c >> 3) * NFFN + fs) * 64 + (c & 7) * 8, pick4(g, dd[0], dd[1], dd[2], dd[3]), tag, lane);
            }
        }
        if (wave == 6 && is_con) {
#pragma unroll
            for (int p = 0; p < 28; ++p) dma(Lb + L_WA + p * 1024, lane * 16, (const char*)(wo + (size_t)j2 * 32 * D) + p * 1024);
            VMCNT(0);
        }
        if (wave == 6 && is_red) {
            for (int z = 0; z < FSLEEP; ++z) __builtin_amdgcn_s_sleep(127);
            unsigned off[7];
#pragma unroll
            for (int k = 0; k < 7; ++k) off[k] = WS_FG + (unsigned)b * NFFN * 64 + k * 1024 + lane * 16;
            v4u v[7];
            gather<7, 64>(rws, off, tag, v);
            float s0 = 0.f, s1 = 0.f;
#pragma unroll
            for (int k = 0; k < 7; ++k) { s0 += __uint_as_float(v[k].x); s1 += __uint_as_float(v[k].z); }
            s0 += dpp<ROR4>(s0); s0 += dpp<ROR8>(s0); s0 = sum32(sum16(s0));
            s1 += dpp<ROR4>(s1); s1 += dpp<ROR8>(s1); s1 = sum32(sum16(s1));
            if (lane < 4) {
                LAS float* fx = (LAS float*)(L + L_FX);
                fx[2 * lane] = rbf(s0); fx[2 * lane + 1] = rbf(s1);
                const unsigned xw = *(const GAS unsigned*)(x + b * 8 + 2 * lane);
                fx[8 + 2 * lane] = bflo(xw); fx[8 + 2 * lane + 1] = bfhi(xw);
            }
        }
        if (wave == 7) {
            for (int z = 0; z < AD_WAIT; ++z) __builtin_amdgcn_s_sleep(127);
            const unsigned offp[1] = {WS_PG + (unsigned)((b & 3) * 64 + lane) * 16};
            v4u vp[1];
            gather<1, 48>(rws, offp, tag, vp);
            const unsigned ref = (unsigned)__builtin_amdgcn_s_memrealtime();
            const float ma = wave_sum((float)(int)(vp[0].x - ref)) * (1.f / 64), mb = wave_sum((float)(int)(vp[0].z - ref)) * (1.f / 64);
            if (lane == 0) *(LAS v4u*)(L + L_SCHED) = v4u{ref + (int)mb + AD_MARGIN, (unsigned)((mb - ma) * (16.f * AD_SCALE / 100.f / (AD_UB - AD_UA))), tag, 0u};
        }
        STAMP(6);
    }
    WG_BARRIER();
    if (wave < 4) STAMP(5);
    if (wave < NSW) {
        const int hh = tid >> 7, d = (tid & 127) * 2;
        float mw[NSW], lw[NSW], a0[NSW], a1[NSW];
#pragma unroll
        for (int w = 0; w < NSW; ++w) {
            const LAS float* part = (const LAS float*)(L + w * 32768);
            const v2u ml = *(const LAS v2u*)(part + 512 + 2 * hh), a2 = *(const LAS v2u*)(part + hh * 256 + d);
            mw[w] = __uint_as_float(ml.x); lw[w] = __uint_as_float(ml.y); a0[w] = __uint_as_float(a2.x); a1[w] = __uint_as_float(a2.y);
        }
        const float M = fmaxf(fmaxf(mw[0], mw[1]), fmaxf(mw[2], mw[3]));
        float A0 = 0.f, A1 = 0.f, Ls = 0.f;
#pragma unroll
        for (int w = 0; w < NSW; ++w) { const float f = ex2((mw[w] - M) * SC2); A0 = fmaf(f, a0[w], A0); A1 = fmaf(f, a1[w], A1); Ls = fmaf(f, lw[w], Ls); }
        const int hq = 2 * h + hh;
        if (d == 0) st16_sc1(rws, WS_ML + (unsigned)(hq * 32 + s) * 16, v4u{__float_as_uint(M), tag, __float_as_uint(Ls), tag});
        st16_sc1(rws, WS_AG + (unsigned)(((hq * 16 + (d >> 4)) * 32 + s) * 16 + (d & 15)) * 8, v4u{__float_as_uint(A0), tag, __float_as_uint(A1), tag});
    }
    if (wave < 4) STAMP(6);
    if (is_con) {
        v8s wb[4];
        if (wave < 7) {
            LAS char* bp = L + L_WA + (8 * g + (i16 >> 2)) * (D * 2) + (i16 & 3) * 8 + wave * 128;
#pragma unroll
            for (int t = 0; t < 4; ++t) {
                const v4s b0 = __builtin_amdgcn_ds_read_tr16_b64_v4i16((LAS v4s*)(bp + t * 32));
                const v4s b1 = __builtin_amdgcn_ds_read_tr16_b64_v4i16((LAS v4s*)(bp + t * 32 + 4 * D * 2));
                wb[t] = __builtin_shufflevector(b0, b1, 0, 1, 2, 3, 4, 5, 6, 7);
            }
        }
        if (wave == 4) {
            const unsigned offm[1] = {WS_ML + (unsigned)((j2 >> 3) * 32 + (lane & 31)) * 16};
            v4u vm[1];
            __builtin_amdgcn_s_sleep(H1DELAY);
            gather<1, MLSLEEP>(rws, offm, tag, vm);
            unsigned off[8];
#pragma unroll
            for (int k = 0; k < 8; ++k) off[k] = WS_AG + (unsigned)j2 * 8192 + k * 1024 + lane * 16;
            v4u v[8];
#pragma unroll
            for (int k = 0; k < 8; ++k) v[k] = ld16_sc1(rws, off[k]);
            asm volatile("" ::: "memory");
            const float ms = __uint_as_float(vm[0].x);
            float M = fmaxf(ms, dpp<XOR1>(ms)); M = fmaxf(M, dpp<XOR2>(M)); M = fmaxf(M, dpp<ROR4>(M)); M = max16(fmaxf(M, dpp<ROR8>(M)));
            const float f = ex2((ms - M) * SC2);
            float Ls = f * __uint_as_float(vm[0].z);
            Ls += dpp<XOR1>(Ls); Ls += dpp<XOR2>(Ls); Ls += dpp<ROR4>(Ls); Ls += dpp<ROR8>(Ls); Ls = sum16(Ls);
            const int wn = __float_as_int(f * __builtin_amdgcn_rcpf(Ls));
            float wk[4];
#pragma unroll
            for (int k = 0; k < 4; ++k) wk[k] = __int_as_float(__builtin_amdgcn_ds_bpermute(((lane >> 3) + 8 * k) * 4, wn));
            bool ok = true;
#pragma unroll
            for (int k = 0; k < 8; ++k) ok &= (v[k].y == tag) & (v[k].w == tag);
            if (!__all(ok)) gather<8, 1>(rws, off, tag, v);
#pragma unroll
            for (int c = 0; c < 2; ++c) {
                float o0 = 0.f, o1 = 0.f;
#pragma unroll
                for (int k = 0; k < 4; ++k) { o0 = fmaf(wk[k], __uint_as_float(v[4 * c + k].x), o0); o1 = fmaf(wk[k], __uint_as_float(v[4 * c + k].z), o1); }
                o0 += dpp<ROR8>(o0); o1 += dpp<ROR8>(o1);
                o0 = sum32(sum16(o0)); o1 = sum32(sum16(o1));
                if (lane < 8) *(LAS unsigned*)(L + L_O + c * 32 + lane * 4) = pk2(o0, o1);
            }
            STAMP(7);
        }
        WG_BARRIER();
        if (wave < 7) {
            const v8bf oa = *(const LAS v8bf*)(L + L_O + g * 16);
            float dd[4];
#pragma unroll
            for (int t = 0; t < 4; ++t) { const v4f z = {0.f, 0.f, 0.f, 0.f}; dd[t] = __builtin_amdgcn_mfma_f32_16x16x32_bf16(oa, __builtin_bit_cast(v8bf, wb[t]), z, 0, 0, 0)[0]; }
            const int c = 64 * wave + lane;
            publish_pair(rws, WS_OG + (unsigned)((c >> 3) * NCON + j2) * 64 + (c & 7) * 8, pick4(g, dd[0], dd[1], dd[2], dd[3]), tag, lane);
        }
    }
    if (is_red) {
        const unsigned off[1] = {WS_OG + (unsigned)b * (NCON * 64) + wave * 1024 + lane * 16};
        v4u v[1];
        __builtin_amdgcn_s_sleep(H2DELAY);
        gather<1, 1>(rws, off, tag, v);
        float s0 = __uint_as_float(v[0].x), s1 = __uint_as_float(v[0].z);
        s0 += dpp<ROR4>(s0); s0 += dpp<ROR8>(s0); s0 = sum32(sum16(s0));
        s1 += dpp<ROR4>(s1); s1 += dpp<ROR8>(s1); s1 = sum32(sum16(s1));
        if (lane < 4) { LAS float* red = (LAS float*)(L + L_RED) + wave * 8; red[2 * lane] = s0; red[2 * lane + 1] = s1; }
        WG_BARRIER();
        if (tid < 8) {
            const LAS float* red = (const LAS float*)(L + L_RED);
            float a = 0.f;
#pragma unroll
            for (int w = 0; w < 8; ++w) a += red[w * 8 + tid];
            const LAS float* fx = (const LAS float*)(L + L_FX);
            const float y = rbf(fx[8 + tid] + rbf(a)) + fx[tid];
            out[b * 8 + tid] = __builtin_bit_cast(u16, (__bf16)y);
        }
        if (b == 0 && tid == 0) __hip_atomic_store((GAS unsigned*)(ws + WS_EPOCH), tag, __ATOMIC_RELAXED, __HIP_MEMORY_SCOPE_AGENT);
        if (wave == 7) STAMP(7);
    }
    STAMP_FLUSH();
}

}

extern "C" void kernel_launch(void* const* d_in, const int* in_sizes, int n_in, void* d_out, int out_size, void* d_ws, size_t ws_size,
                              hipStream_t stream) {
    (void)in_sizes; (void)n_in; (void)out_size; (void)ws_size;
    hipLaunchKernelGGL(fused_decode_kernel, dim3(NWG), dim3(NTHR), 0, stream,
                       (const u16*)d_in[0], (const u16*)d_in[1], (const u16*)d_in[2], (const u16*)d_in[3], (const u16*)d_in[4],
                       (const u16*)d_in[6], (const u16*)d_in[7], (u16*)d_out, (unsigned char*)d_ws);
}
```

```cpp
#include <hip/hip_runtime.h>
#include <hip/hip_bf16.h>
#include <stdint.h>

#define GAS __attribute__((address_space(1)))
#define LAS __attribute__((address_space(3)))

namespace {

typedef unsigned short u16;
typedef unsigned v4u __attribute__((ext_vector_type(4)));
typedef unsigned v2u __attribute__((ext_vector_type(2)));
typedef float v4f __attribute__((ext_vector_type(4)));
typedef short v4s __attribute__((ext_vector_type(4)));
typedef short v8s __attribute__((ext_vector_type(8)));
typedef __bf16 v8bf __attribute__((ext_vector_type(8)));
typedef __bf16 bf16x2 __attribute__((ext_vector_type(2)));

constexpr int D = 448, SK = 32768, DFF = 1792, NQ = 4096;
constexpr int NWG = 256, NTHR = 512;
constexpr int NSPLIT = 32;
constexpr int NSW = 4;
constexpr int TILES = SK / NSPLIT / NSW / 16;
constexpr int NFFN = DFF / 16;
constexpr int NRED = D / 8;
constexpr int NCON = 128;
constexpr float SC2 = 0.0625f * 1.4426950408889634f;
constexpr unsigned SPIN_CAP = 1u << 18;
static_assert(TILES == 16, "the phase list below is written out for 16 tiles");

constexpr int NFLY = 6, BOOST_X = 2;
constexpr int AD_UA = 6, AD_UB = 11;
constexpr int AD_MARGIN = 28, AD_AHEAD = 15;
constexpr int AD_WAIT = 4;
constexpr int QDELAY = 40, QSLEEP = 4, HDELAY = 60, FSLEEP = 3;
constexpr int H1DELAY = 22, MLSLEEP = 4, H2DELAY = 40;
constexpr int TMASK8 = 12;

constexpr unsigned WS_EPOCH = 0;
constexpr unsigned WS_QG = 4096;
constexpr unsigned WS_ML = WS_QG + 8 * 4 * 512 * 8;
constexpr unsigned WS_AG = WS_ML + 16 * 32 * 16;
constexpr unsigned WS_OG = WS_AG + 256 * 4096;
constexpr unsigned WS_FG = WS_OG + NRED * NCON * 64;
constexpr unsigned WS_HG = WS_FG + NRED * NFFN * 64;
constexpr unsigned WS_PG = WS_HG + 28 * 4 * 64 * 8;
constexpr unsigned WS_END = WS_PG + 256 * 16;

constexpr unsigned L_Q = 131072;
constexpr unsigned L_XN = L_Q + 1024;
constexpr unsigned L_WA = L_XN + 896;
constexpr unsigned L_WB = L_WA + 14336;
constexpr unsigned L_O = L_WB + 14336;
constexpr unsigned L_RED = L_O + 64;
constexpr unsigned L_FX = L_RED + 256;
constexpr unsigned L_TAG = L_FX + 64;
constexpr unsigned L_SCHED = L_TAG + 16;
constexpr unsigned L_END = L_SCHED + 16;

__device__ __forceinline__ float bflo(unsigned w) { return __uint_as_float(w << 16); }
__device__ __forceinline__ float bfhi(unsigned w) { return __uint_as_float(w & 0xffff0000u); }
__device__ __forceinline__ float rbf(float f) { return (float)(__bf16)f; }
__device__ __forceinline__ unsigned pk2(float a, float b) { bf16x2 v = {(__bf16)a, (__bf16)b}; return __builtin_bit_cast(unsigned, v); }
template <int CTRL> __device__ __forceinline__ float dpp(float x) {
    return __builtin_bit_cast(float, __builtin_amdgcn_mov_dpp(__builtin_bit_cast(int, x), CTRL, 0xf, 0xf, true));
}
constexpr int XOR1 = 0xB1, XOR2 = 0x4E, ROR4 = 0x124, ROR8 = 0x128;
__device__ __forceinline__ float sum16(float x) {
    auto s = __builtin_amdgcn_permlane16_swap(__float_as_uint(x), __float_as_uint(x), false, false);
    return __uint_as_float(s[0]) + __uint_as_float(s[1]);
}
__device__ __forceinline__ float sum32(float x) {
    auto s = __builtin_amdgcn_permlane32_swap(__float_as_uint(x), __float_as_uint(x), false, false);
    return __uint_as_float(s[0]) + __uint_as_float(s[1]);
}
__device__ __forceinline__ float max16(float x) {
    auto s = __builtin_amdgcn_permlane16_swap(__float_as_uint(x), __float_as_uint(x), false, false);
    return fmaxf(__uint_as_float(s[0]), __uint_as_float(s[1]));
}
__device__ __forceinline__ float max32(float x) {
    auto s = __builtin_amdgcn_permlane32_swap(__float_as_uint(x), __float_as_uint(x), false, false);
    return fmaxf(__uint_as_float(s[0]), __uint_as_float(s[1]));
}
__device__ __forceinline__ float wave_sum(float x) {
    x += dpp<XOR1>(x); x += dpp<XOR2>(x); x += dpp<ROR4>(x); x += dpp<ROR8>(x);
    return sum32(sum16(x));
}
__device__ __forceinline__ float ex2(float x) { return __builtin_amdgcn_exp2f(x); }

#define WG_BARRIER() do { asm volatile("s_waitcnt lgkmcnt(0)" ::: "memory"); __builtin_amdgcn_s_barrier(); asm volatile("" ::: "memory"); } while (0)
#define VMCNT(n) asm volatile("s_waitcnt vmcnt(%0)" :: "i"(n) : "memory")
#define SB() __builtin_amdgcn_sched_barrier(0)

__device__ __forceinline__ void dma(unsigned ldsaddr, unsigned voff, const void* gbase) {
    asm volatile("s_mov_b32 m0, %0\n\ts_nop 0\n\tglobal_load_lds_dwordx4 %1, %2 nt" :: "s"(ldsaddr), "v"(voff), "s"(gbase) : "memory");
}
template <int N> __device__ __forceinline__ void dma_win(unsigned ldsaddr, unsigned voff, const void* gbase) {
    asm volatile("s_waitcnt vmcnt(%3)\n\ts_mov_b32 m0, %0\n\ts_nop 0\n\tglobal_load_lds_dwordx4 %1, %2 nt" :: "s"(ldsaddr), "v"(voff), "s"(gbase), "i"(N - 1) : "memory");
}
template <int N, int X> __device__ __forceinline__ void dma_win3(unsigned ldsaddr, unsigned voff, const void* gbase, unsigned level) {
    asm volatile("s_cmp_gt_u32 %4, 1\n\ts_cbranch_scc1 2f\n\ts_cmp_eq_u32 %4, 1\n\ts_cbranch_scc1 1f\n\ts_waitcnt vmcnt(%6)\n1:\n\ts_waitcnt vmcnt(%3)\n2:\n\ts_waitcnt vmcnt(%5)\n\t"
                 "s_mov_b32 m0, %0\n\ts_nop 0\n\tglobal_load_lds_dwordx4 %1, %2 nt"
                 :: "s"(ldsaddr), "v"(voff), "s"(gbase), "i"(N - 1), "s"(level), "i"(N + X - 1), "i"(N - 2) : "memory", "scc");
}

typedef __amdgpu_buffer_rsrc_t rsrc_t;
__device__ __forceinline__ v4u ld16_sc1(rsrc_t r, unsigned off) { return __builtin_amdgcn_raw_buffer_load_b128(r, off, 0, 16); }
__device__ __forceinline__ void st16_sc1(rsrc_t r, unsigned off, v4u v) { __builtin_amdgcn_raw_buffer_store_b128(v, r, off, 0, 16); }

__device__ __forceinline__ void publish_pair(rsrc_t r, unsigned off, float v, unsigned tag, int lane) {
    const float nb = dpp<XOR1>(v);
    if (!(lane & 1)) st16_sc1(r, off, v4u{__float_as_uint(v), tag, __float_as_uint(nb), tag});
}
template <int N, int SLEEP> __device__ __forceinline__ void gather(rsrc_t r, const unsigned (&off)[N], unsigned tag, v4u (&v)[N]) {
    for (unsigned spin = 0; spin < SPIN_CAP; ++spin) {
        asm volatile("" ::: "memory");
        bool ok = true;
#pragma unroll
        for (int k = 0; k < N; ++k) v[k] = ld16_sc1(r, off[k]);
#pragma unroll
        for (int k = 0; k < N; ++k) ok &= (v[k].y == tag) & (v[k].w == tag);
        if (__all(ok)) break;
        __builtin_amdgcn_s_sleep(SLEEP);
    }
}

__device__ __forceinline__ void layernorm_to_lds(v4u xv, LAS char* xn, int lane) {
    float f[8];
#pragma unroll
    for (int k = 0; k < 4; ++k) { f[2 * k] = bflo(xv[k]); f[2 * k + 1] = bfhi(xv[k]); }
    float s = 0.f;
#pragma unroll
    for (int k = 0; k < 8; ++k) s += f[k];
    const float mean = wave_sum(s) * (1.0f / D);
    float v = 0.f;
#pragma unroll
    for (int k = 0; k < 8; ++k) { const float d = f[k] - mean; v = fmaf(d, d, v); }
    if (lane >= 56) v = 0.f;
    const float var = wave_sum(v) * (1.0f / D);
    const float mb = rbf(mean), den = rbf(sqrtf(rbf(rbf(var) + 1e-5f)));
    v4u o;
#pragma unroll
    for (int k = 0; k < 4; ++k) o[k] = pk2(rbf(f[2 * k] - mb) / den, rbf(f[2 * k + 1] - mb) / den);
    if (lane < 56) *(LAS v4u*)(xn + lane * 16) = o;
}

__device__ __forceinline__ float pick4(int g, float a, float b, float c, float d) { return g == 0 ? a : g == 1 ? b : g == 2 ? c : d; }
__device__ __forceinline__ void block_dma(const u16* w, int ld, unsigned ldsaddr, int lane) {
    const unsigned voff = (unsigned)(lane >> 3) * (ld * 2) + (lane & 7) * 16;
#pragma unroll
    for (int i = 0; i < 14; ++i) dma(ldsaddr + i * 1024, voff, (const char*)w + (size_t)i * 8 * ld * 2);
}
__device__ __forceinline__ float block_mfma(LAS char* L, unsigned wofs, int rb, int lane) {
    const int g = lane >> 4, i = lane & 15;
    v4f d[4] = {{0.f, 0.f, 0.f, 0.f}, {0.f, 0.f, 0.f, 0.f}, {0.f, 0.f, 0.f, 0.f}, {0.f, 0.f, 0.f, 0.f}};
    LAS char* ap = L + L_XN + rb * 224 + g * 8;
    LAS char* bp = L + wofs + (4 * g + (i >> 2)) * 128 + (i & 3) * 8;
#pragma unroll
    for (int ks = 0; ks < 7; ++ks) {
        const v4s av = *(const LAS v4s*)(ap + ks * 32);
#pragma unroll
        for (int t = 0; t < 4; ++t) {
            const v4s bv = __builtin_amdgcn_ds_read_tr16_b64_v4i16((LAS v4s*)(bp + ks * 2048 + t * 32));
            d[t] = __builtin_amdgcn_mfma_f32_16x16x16bf16_1k(av, bv, d[t], 0, 0, 0);
        }
    }
    return pick4(g, d[0][0], d[1][0], d[2][0], d[3][0]);
}

__global__ void __launch_bounds__(NTHR, 2)
fused_decode_kernel(const u16* __restrict__ x, const u16* __restrict__ Kc, const u16* __restrict__ Vc, const u16* __restrict__ wq,
                    const u16* __restrict__ wo, const u16* __restrict__ w1, const u16* __restrict__ w2, u16* __restrict__ out,
                    unsigned char* __restrict__ ws) {
    __shared__ __attribute__((aligned(16))) char lds[L_END];
    LAS char* L = (LAS char*)lds;
    asm volatile("" :: "s"(x), "s"(Kc), "s"(Vc), "s"(wq), "s"(wo), "s"(w1), "s"(w2), "s"(out), "s"(ws));
    const int tid = threadIdx.x, lane = tid & 63, wave = __builtin_amdgcn_readfirstlane(tid >> 6);
    const int b = blockIdx.x, h = b & 7, s = b >> 3;
    const int g = lane >> 4, i16 = lane & 15;
    const unsigned Lb = __builtin_amdgcn_readfirstlane((unsigned)(size_t)L);
    const rsrc_t rws = __builtin_amdgcn_make_buffer_rsrc(ws, 0, WS_END, 0x00020000);
    const bool is_ffn = s < 14, is_con = s >= 16, is_red = b < NRED;
    const int j2 = h * 16 + (s - 16);
    unsigned tag = 0;

    if (wave < NSW) {
        const unsigned ring = Lb + wave * 32768;
        const int hf = lane >> 5, cp = lane & 31;
        unsigned voffK[8], voffV[8];
#pragma unroll
        for (int p = 0; p < 8; ++p) { const int key = 2 * p + hf; voffK[p] = key * 512 + ((cp ^ (key & 15)) * 16); voffV[p] = key * 512 + ((cp ^ (4 * (key & 3))) * 16); }
        const char* Kh = (const char*)Kc + (size_t)h * SK * 512 + (size_t)s * (SK / NSPLIT * 512) + (size_t)wave * 8192;
        const char* Vh = (const char*)Vc + (size_t)h * SK * 512 + (size_t)s * (SK / NSPLIT * 512) + (size_t)wave * 8192;
        constexpr size_t TSTRIDE = (size_t)NSW * 8192;
        const int tmask = (s & 8) ? TMASK8 : 0;
#pragma unroll
        for (int t = 0; t < 4; ++t)
#pragma unroll
            for (int p = 0; p < 8; ++p) dma_win<NFLY - 1>(ring + t * 8192 + p * 1024, voffK[p], Kh + (size_t)(t ^ tmask) * TSTRIDE);
        WG_BARRIER();
        tag = *(LAS unsigned*)(L + L_TAG);
        v8bf qf[8];
#pragma unroll
        for (int ks = 0; ks < 8; ++ks) qf[ks] = *(const LAS v8bf*)(L + L_Q + (i16 & 1) * 512 + ks * 64 + g * 16);
        LAS char* R = L + wave * 32768;
        unsigned ka[4], va[2];
#pragma unroll
        for (int k4 = 0; k4 < 4; ++k4) ka[k4] = i16 * 512 + ((k4 ^ (i16 >> 2)) * 64) + ((g ^ (i16 & 3)) * 16);
        const int q4 = (lane >> 2) & 3, p4 = lane & 3;
#pragma unroll
        for (int u1 = 0; u1 < 2; ++u1) va[u1] = q4 * 512 + (((8 * u1 + 2 * g + (p4 >> 1)) ^ (4 * q4)) * 16) + (p4 & 1) * 8;
        v2u sc[TILES];
        float mx = -INFINITY, m = 0.f, l = 0.f;
        v4f acc[4] = {{0.f, 0.f, 0.f, 0.f}, {0.f, 0.f, 0.f, 0.f}, {0.f, 0.f, 0.f, 0.f}, {0.f, 0.f, 0.f, 0.f}};
        unsigned level = 0, t_a = 0;

#define PIECE(u, p) do { if ((u) >= 1 && (u) <= 28) { \
            if ((u) + 3 < TILES) dma_win3<NFLY, BOOST_X>(ring + (((u) + 3) & 3) * 8192 + (p) * 1024, voffK[p], gb, level); \
            else dma_win3<NFLY, BOOST_X>(ring + (((u) + 3) & 3) * 8192 + (p) * 1024, voffV[p], gb, level); } SB(); } while (0)
#define PACE(u) do { if ((u) >= 1 && (u) <= 28) { \
            if ((u) == AD_UA || (u) >= AD_UB) { const unsigned now_ = (unsigned)__builtin_amdgcn_s_memrealtime(); \
                if ((u) == AD_UA) t_a = now_; \
                if ((u) == AD_UB && wave == 0 && lane == 0) st16_sc1(rws, WS_PG + b * 16, v4u{t_a, tag, now_, tag}); \
                if ((u) > AD_UB) { const v4u sc_ = *(const LAS v4u*)(L + L_SCHED); \
                    const int lag_ = (int)(now_ - (sc_.x + ((((u) - AD_UB) * sc_.y) >> 4))); \
                    level = __builtin_amdgcn_readfirstlane(sc_.z != tag ? 0u : lag_ > 0 ? 2u : lag_ < -AD_AHEAD ? 0u : 1u); } } } } while (0)
#define STEP_BASE(u) const char* gb = ((u) + 3 < TILES ? Kh + (size_t)(((u) + 3) ^ tmask) * TSTRIDE : Vh + (size_t)((((u) + 3 - TILES) & 15) ^ tmask) * TSTRIDE)
#define LANDED(u) asm volatile("s_waitcnt vmcnt(%0) lgkmcnt(0)" :: "i"((u) == 0 ? 24 : (u) <= 29 ? 16 : (u) == 30 ? 8 : 0) : "memory")
#define SOFTMAX(t) do { \
            const float p0 = ex2((bflo(sc[t].x) - m) * SC2), p1 = ex2((bfhi(sc[t].x) - m) * SC2), p2 = ex2((bflo(sc[t].y) - m) * SC2), p3 = ex2((bfhi(sc[t].y) - m) * SC2); \
            l += (p0 + p1) + (p2 + p3); \
            sc[t] = v2u{pk2(p0, p1), pk2(p2, p3)}; } while (0)
#define PHASE_K(u) do { \
            PACE(u); LANDED(u); STEP_BASE(u); \
            v8bf kf[8]; \
            _Pragma("unroll") for (int ks = 0; ks < 8; ++ks) kf[ks] = *(const LAS v8bf*)(R + ((u) & 3) * 8192 + (ks >> 2) * 256 + ka[ks & 3]); \
            SB(); PIECE(u, 0); \
            v4f d = {0.f, 0.f, 0.f, 0.f}; \
            _Pragma("unroll") for (int ks = 0; ks < 4; ++ks) d = __builtin_amdgcn_mfma_f32_16x16x32_bf16(kf[ks], qf[ks], d, 0, 0, 0); \
            SB(); PIECE(u, 1); \
            _Pragma("unroll") for (int ks = 4; ks < 8; ++ks) d = __builtin_amdgcn_mfma_f32_16x16x32_bf16(kf[ks], qf[ks], d, 0, 0, 0); \
            SB(); PIECE(u, 2); \
            sc[u] = v2u{pk2(d[0], d[1]), pk2(d[2], d[3])}; \
            mx = fmaxf(fmaxf(mx, fmaxf(d[0], d[1])), fmaxf(d[2], d[3])); \
            SB(); PIECE(u, 3); \
            if ((u) == TILES - 1) { m = max32(max16(rbf(mx))); SB(); PIECE(u, 4); SOFTMAX(0); SB(); } else PIECE(u, 4); \
            PIECE(u, 5); PIECE(u, 6); PIECE(u, 7); \
        } while (0)
#define PV_RD(kap, u) _Pragma("unroll") for (int c = 0; c < 4; ++c) \
            vf[kap][c] = __builtin_amdgcn_ds_read_tr16_b64_v4i16((LAS v4s*)(R + ((u) & 3) * 8192 + (kap) * 2048 + (c >> 1) * 256 + va[c & 1]));
#define PV_MM(kap) _Pragma("unroll") for (int c = 0; c < 4; ++c) acc[c] = __builtin_amdgcn_mfma_f32_4x4x4bf16_1k(P, vf[kap][c], acc[c], 4, 4 * (kap), 0);
#define PHASE_V(u) do { \
            PACE(u); LANDED(u); STEP_BASE(u); \
            const v4s P = __builtin_bit_cast(v4s, sc[(u) - TILES]); \
            v4s vf[4][4]; \
            PV_RD(0, u) PV_RD(1, u) SB(); PIECE(u, 0); \
            PV_RD(2, u) PV_RD(3, u) SB(); PIECE(u, 1); \
            PV_MM(0) SB(); PIECE(u, 2); \
            PV_MM(1) SB(); PIECE(u, 3); \
            PV_MM(2) SB(); PIECE(u, 4); \
            PV_MM(3) SB(); PIECE(u, 5); \
            if ((u) < 31) { SOFTMAX(((u) - TILES + 1) & 15); SB(); } \
            PIECE(u, 6); PIECE(u, 7); \
        } while (0)
        PHASE_K(0); PHASE_K(1); PHASE_K(2); PHASE_K(3); PHASE_K(4); PHASE_K(5); PHASE_K(6); PHASE_K(7);
        PHASE_K(8); PHASE_K(9); PHASE_K(10); PHASE_K(11); PHASE_K(12); PHASE_K(13); PHASE_K(14); PHASE_K(15);
        PHASE_V(16); PHASE_V(17); PHASE_V(18); PHASE_V(19); PHASE_V(20); PHASE_V(21); PHASE_V(22); PHASE_V(23);
        PHASE_V(24); PHASE_V(25); PHASE_V(26); PHASE_V(27); PHASE_V(28); PHASE_V(29); PHASE_V(30); PHASE_V(31);
        l = sum32(sum16(l));
        LAS float* part = (LAS float*)R;
#pragma unroll
        for (int u = 0; u < 4; ++u) { part[64 * u + lane] = acc[u][0]; part[256 + 64 * u + lane] = acc[u][1]; }
        if (lane < 2) { part[512 + 2 * lane] = m; part[513 + 2 * lane] = l; }
    } else {
        if (wave == 4) {
            const int rb = s & 3, cb = s >> 2;
            v4u xv; unsigned ep;
            const u16* xp = x + (lane < 56 ? lane : 55) * 8;
            asm volatile("global_load_dwordx4 %0, %1, off" : "=v"(xv) : "v"(xp) : "memory");
            asm volatile("global_load_dword %0, %1, off sc1" : "=v"(ep) : "v"(ws + WS_EPOCH) : "memory");
            block_dma(wq + (size_t)rb * 112 * NQ + h * 512 + cb * 64, NQ, Lb + L_WA, lane);
            asm volatile("s_waitcnt vmcnt(15)" : "+v"(xv) :: "memory");
            if (lane >= 56) xv = v4u{0u, 0u, 0u, 0u};
            layernorm_to_lds(xv, L + L_XN, lane);
            asm volatile("s_waitcnt vmcnt(0) lgkmcnt(0)" : "+v"(ep) :: "memory");
            tag = ep + 1u;
            publish_pair(rws, WS_QG + (unsigned)((h * 4 + rb) * 512 + cb * 64 + lane) * 8, block_mfma(L, L_WA, rb, lane), tag, lane);
            unsigned off[16];
#pragma unroll
            for (int k = 0; k < 4; ++k)
#pragma unroll
                for (int r = 0; r < 4; ++r) off[4 * k + r] = WS_QG + (unsigned)((h * 4 + r) * 512 + 128 * k + 2 * lane) * 8;
            v4u v[16];
            __builtin_amdgcn_s_sleep(QDELAY);
            gather<16, QSLEEP>(rws, off, tag, v);
#pragma unroll
            for (int k = 0; k < 4; ++k) {
                const float q0 = (__uint_as_float(v[4 * k].x) + __uint_as_float(v[4 * k + 1].x)) + (__uint_as_float(v[4 * k + 2].x) + __uint_as_float(v[4 * k + 3].x));
                const float q1 = (__uint_as_float(v[4 * k].z) + __uint_as_float(v[4 * k + 1].z)) + (__uint_as_float(v[4 * k + 2].z) + __uint_as_float(v[4 * k + 3].z));
                *(LAS unsigned*)(L + L_Q + (64 * k + lane) * 4) = pk2(q0, q1);
            }
            if (lane == 0) *(LAS unsigned*)(L + L_TAG) = tag;
        }
        WG_BARRIER();
        tag = *(LAS unsigned*)(L + L_TAG);
        if (wave == 5 && is_ffn) {
            const int fs = h * 14 + s, cb = fs >> 2, rb = fs & 3;
            block_dma(w1 + (size_t)rb * 112 * DFF + cb * 64, DFF, Lb + L_WB, lane);
#pragma unroll
            for (int p = 0; p < 14; ++p) dma(Lb + L_WA + p * 1024, lane * 16, (const char*)(w2 + (size_t)fs * 16 * D) + p * 1024);
            VMCNT(14);
            publish_pair(rws, WS_HG + (unsigned)((cb * 4 + rb) * 64 + lane) * 8, block_mfma(L, L_WB, rb, lane), tag, lane);
            const unsigned offh[1] = {WS_HG + (unsigned)((cb * 4 + ((lane >> 3) & 3)) * 64 + 16 * rb + 2 * (lane & 7)) * 8};
            v4u vh[1];
            __builtin_amdgcn_s_sleep(HDELAY);
            gather<1, 16>(rws, offh, tag, vh);
            float h0 = __uint_as_float(vh[0].x), h1 = __uint_as_float(vh[0].z);
            h0 += dpp<ROR8>(h0); h1 += dpp<ROR8>(h1); h0 = sum16(h0); h1 = sum16(h1);
            if (lane < 8) *(LAS unsigned*)(L + L_O + lane * 4) = pk2(fmaxf(rbf(h0), 0.f), fmaxf(rbf(h1), 0.f));
            asm volatile("s_waitcnt vmcnt(0) lgkmcnt(0)" ::: "memory");
            const v4s ha = *(const LAS v4s*)(L + L_O + g * 8);
            LAS char* bp = L + L_WA + (4 * g + (i16 >> 2)) * (D * 2) + (i16 & 3) * 8;
#pragma unroll
            for (int rr = 0; rr < 7; ++rr) {
                float dd[4];
#pragma unroll
                for (int t = 0; t < 4; ++t) {
                    const v4s bv = __builtin_amdgcn_ds_read_tr16_b64_v4i16((LAS v4s*)(bp + (4 * rr + t) * 32));
                    const v4f z = {0.f, 0.f, 0.f, 0.f};
                    dd[t] = __builtin_amdgcn_mfma_f32_16x16x16bf16_1k(ha, bv, z, 0, 0, 0)[0];
                }
                const int c = 64 * rr + lane;
                publish_pair(rws, WS_FG + (unsigned)((c >> 3) * NFFN + fs) * 64 + (c & 7) * 8, pick4(g, dd[0], dd[1], dd[2], dd[3]), tag, lane);
            }
        }
        if (wave == 6 && is_con) {
#pragma unroll
            for (int p = 0; p < 28; ++p) dma(Lb + L_WA + p * 1024, lane * 16, (const char*)(wo + (size_t)j2 * 32 * D) + p * 1024);
            VMCNT(0);
        }
        if (wave == 6 && is_red) {
            for (int z = 0; z < FSLEEP; ++z) __builtin_amdgcn_s_sleep(127);
            unsigned off[7];
#pragma unroll
            for (int k = 0; k < 7; ++k) off[k] = WS_FG + (unsigned)b * NFFN * 64 + k * 1024 + lane * 16;
            v4u v[7];
            gather<7, 64>(rws, off, tag, v);
            float s0 = 0.f, s1 = 0.f;
#pragma unroll
            for (int k = 0; k < 7; ++k) { s0 += __uint_as_float(v[k].x); s1 += __uint_as_float(v[k].z); }
            s0 += dpp<ROR4>(s0); s0 += dpp<ROR8>(s0); s0 = sum32(sum16(s0));
            s1 += dpp<ROR4>(s1); s1 += dpp<ROR8>(s1); s1 = sum32(sum16(s1));
            if (lane < 4) {
                LAS float* fx = (LAS float*)(L + L_FX);
                fx[2 * lane] = rbf(s0); fx[2 * lane + 1] = rbf(s1);
                const unsigned xw = *(const GAS unsigned*)(x + b * 8 + 2 * lane);
                fx[8 + 2 * lane] = bflo(xw); fx[8 + 2 * lane + 1] = bfhi(xw);
            }
        }
        if (wave == 7) {
            for (int z = 0; z < AD_WAIT; ++z) __builtin_amdgcn_s_sleep(127);
            const unsigned offp[1] = {WS_PG + (unsigned)((b & 3) * 64 + lane) * 16};
            v4u vp[1];
            gather<1, 48>(rws, offp, tag, vp);
            const unsigned ref = (unsigned)__builtin_amdgcn_s_memrealtime();
            const float ma = wave_sum((float)(int)(vp[0].x - ref)) * (1.f / 64), mb = wave_sum((float)(int)(vp[0].z - ref)) * (1.f / 64);
            if (lane == 0) *(LAS v4u*)(L + L_SCHED) = v4u{ref + (int)mb + AD_MARGIN, (unsigned)((mb - ma) * (16.f / (AD_UB - AD_UA))), tag, 0u};
        }
    }
    WG_BARRIER();
    if (wave < NSW) {
        const int hh = tid >> 7, d = (tid & 127) * 2;
        float mw[NSW], lw[NSW], a0[NSW], a1[NSW];
#pragma unroll
        for (int w = 0; w < NSW; ++w) {
            const LAS float* part = (const LAS float*)(L + w * 32768);
            const v2u ml = *(const LAS v2u*)(part + 512 + 2 * hh), a2 = *(const LAS v2u*)(part + hh * 256 + d);
            mw[w] = __uint_as_float(ml.x); lw[w] = __uint_as_float(ml.y); a0[w] = __uint_as_float(a2.x); a1[w] = __uint_as_float(a2.y);
        }
        const float M = fmaxf(fmaxf(mw[0], mw[1]), fmaxf(mw[2], mw[3]));
        float A0 = 0.f, A1 = 0.f, Ls = 0.f;
#pragma unroll
        for (int w = 0; w < NSW; ++w) { const float f = ex2((mw[w] - M) * SC2); A0 = fmaf(f, a0[w], A0); A1 = fmaf(f, a1[w], A1); Ls = fmaf(f, lw[w], Ls); }
        const int hq = 2 * h + hh;
        if (d == 0) st16_sc1(rws, WS_ML + (unsigned)(hq * 32 + s) * 16, v4u{__float_as_uint(M), tag, __float_as_uint(Ls), tag});
        st16_sc1(rws, WS_AG + (unsigned)(((hq * 16 + (d >> 4)) * 32 + s) * 16 + (d & 15)) * 8, v4u{__float_as_uint(A0), tag, __float_as_uint(A1), tag});
    }
    if (is_con) {
        v8s wb[4];
        if (wave < 7) {
            LAS char* bp = L + L_WA + (8 * g + (i16 >> 2)) * (D * 2) + (i16 & 3) * 8 + wave * 128;
#pragma unroll
            for (int t = 0; t < 4; ++t) {
                const v4s b0 = __builtin_amdgcn_ds_read_tr16_b64_v4i16((LAS v4s*)(bp + t * 32));
                const v4s b1 = __builtin_amdgcn_ds_read_tr16_b64_v4i16((LAS v4s*)(bp + t * 32 + 4 * D * 2));
                wb[t] = __builtin_shufflevector(b0, b1, 0, 1, 2, 3, 4, 5, 6, 7);
            }
        }
        if (wave == 4) {
            const unsigned offm[1] = {WS_ML + (unsigned)((j2 >> 3) * 32 + (lane & 31)) * 16};
            v4u vm[1];
            __builtin_amdgcn_s_sleep(H1DELAY);
            gather<1, MLSLEEP>(rws, offm, tag, vm);
            unsigned off[8];
#pragma unroll
            for (int k = 0; k < 8; ++k) off[k] = WS_AG + (unsigned)j2 * 8192 + k * 1024 + lane * 16;
            v4u v[8];
#pragma unroll
            for (int k = 0; k < 8; ++k) v[k] = ld16_sc1(rws, off[k]);
            asm volatile("" ::: "memory");
            const float ms = __uint_as_float(vm[0].x);
            float M = fmaxf(ms, dpp<XOR1>(ms)); M = fmaxf(M, dpp<XOR2>(M)); M = fmaxf(M, dpp<ROR4>(M)); M = max16(fmaxf(M, dpp<ROR8>(M)));
            const float f = ex2((ms - M) * SC2);
            float Ls = f * __uint_as_float(vm[0].z);
            Ls += dpp<XOR1>(Ls); Ls += dpp<XOR2>(Ls); Ls += dpp<ROR4>(Ls); Ls += dpp<ROR8>(Ls); Ls = sum16(Ls);
            const int wn = __float_as_int(f * __builtin_amdgcn_rcpf(Ls));
            float wk[4];
#pragma unroll
            for (int k = 0; k < 4; ++k) wk[k] = __int_as_float(__builtin_amdgcn_ds_bpermute(((lane >> 3) + 8 * k) * 4, wn));
            bool ok = true;
#pragma unroll
            for (int k = 0; k < 8; ++k) ok &= (v[k].y == tag) & (v[k].w == tag);
            if (!__all(ok)) gather<8, 1>(rws, off, tag, v);
#pragma unroll
            for (int c = 0; c < 2; ++c) {
                float o0 = 0.f, o1 = 0.f;
#pragma unroll
                for (int k = 0; k < 4; ++k) { o0 = fmaf(wk[k], __uint_as_float(v[4 * c + k].x), o0); o1 = fmaf(wk[k], __uint_as_float(v[4 * c + k].z), o1); }
                o0 += dpp<ROR8>(o0); o1 += dpp<ROR8>(o1);
                o0 = sum32(sum16(o0)); o1 = sum32(sum16(o1));
                if (lane < 8) *(LAS unsigned*)(L + L_O + c * 32 + lane * 4) = pk2(o0, o1);
            }
        }
        WG_BARRIER();
        if (wave < 7) {
            const v8bf oa = *(const LAS v8bf*)(L + L_O + g * 16);
            float dd[4];
#pragma unroll
            for (int t = 0; t < 4; ++t) { const v4f z = {0.f, 0.f, 0.f, 0.f}; dd[t] = __builtin_amdgcn_mfma_f32_16x16x32_bf16(oa, __builtin_bit_cast(v8bf, wb[t]), z, 0, 0, 0)[0]; }
            const int c = 64 * wave + lane;
            publish_pair(rws, WS_OG + (unsigned)((c >> 3) * NCON + j2) * 64 + (c & 7) * 8, pick4(g, dd[0], dd[1], dd[2], dd[3]), tag, lane);
        }
    }
    if (is_red) {
        const unsigned off[1] = {WS_OG + (unsigned)b * (NCON * 64) + wave * 1024 + lane * 16};
        v4u v[1];
        __builtin_amdgcn_s_sleep(H2DELAY);
        gather<1, 1>(rws, off, tag, v);
        float s0 = __uint_as_float(v[0].x), s1 = __uint_as_float(v[0].z);
        s0 += dpp<ROR4>(s0); s0 += dpp<ROR8>(s0); s0 = sum32(sum16(s0));
        s1 += dpp<ROR4>(s1); s1 += dpp<ROR8>(s1); s1 = sum32(sum16(s1));
        if (lane < 4) { LAS float* red = (LAS float*)(L + L_RED) + wave * 8; red[2 * lane] = s0; red[2 * lane + 1] = s1; }
        WG_BARRIER();
        if (tid < 8) {
            const LAS float* red = (const LAS float*)(L + L_RED);
            float a = 0.f;
#pragma unroll
            for (int w = 0; w < 8; ++w) a += red[w * 8 + tid];
            const LAS float* fx = (const LAS float*)(L + L_FX);
            const float y = rbf(fx[8 + tid] + rbf(a)) + fx[tid];
            out[b * 8 + tid] = __builtin_bit_cast(u16, (__bf16)y);
        }
        if (b == 0 && tid == 0) __hip_atomic_store((GAS unsigned*)(ws + WS_EPOCH), tag, __ATOMIC_RELAXED, __HIP_MEMORY_SCOPE_AGENT);
    }
}

}

extern "C" void kernel_launch(void* const* d_in, const int* in_sizes, int n_in, void* d_out, int out_size, void* d_ws, size_t ws_size,
                              hipStream_t stream) {
    (void)in_sizes; (void)n_in; (void)out_size; (void)ws_size;
    hipLaunchKernelGGL(fused_decode_kernel, dim3(NWG), dim3(NTHR), 0, stream,
                       (const u16*)d_in[0], (const u16*)d_in[1], (const u16*)d_in[2], (const u16*)d_in[3], (const u16*)d_in[4],
                       (const u16*)d_in[6], (const u16*)d_in[7], (u16*)d_out, (unsigned char*)d_ws);
}
```

```cpp
#include <hip/hip_runtime.h>
#include <hip/hip_bf16.h>
#include <stdint.h>

#define GAS __attribute__((address_space(1)))
#define LAS __attribute__((address_space(3)))

namespace {

typedef unsigned short u16;
typedef unsigned v4u __attribute__((ext_vector_type(4)));
typedef unsigned v2u __attribute__((ext_vector_type(2)));
typedef float v4f __attribute__((ext_vector_type(4)));
typedef short v4s __attribute__((ext_vector_type(4)));
typedef short v8s __attribute__((ext_vector_type(8)));
typedef __bf16 v8bf __attribute__((ext_vector_type(8)));
typedef __bf16 bf16x2 __attribute__((ext_vector_type(2)));

constexpr int D = 448, SK = 32768, DFF = 1792, NQ = 4096;
constexpr int NWG = 256, NTHR = 512;
constexpr int NSPLIT = 32;
constexpr int NSW = 4;
constexpr int TILES = SK / NSPLIT / NSW / 16;
constexpr int NFFN = DFF / 16;
constexpr int NRED = D / 8;
constexpr int NCON = 128;
constexpr float SC2 = 0.0625f * 1.4426950408889634f;
constexpr unsigned SPIN_CAP = 1u << 18;
static_assert(TILES == 16, "the phase list below is written out for 16 tiles");

constexpr int NFLY = 6, BOOST_X = 2;
constexpr int AD_UA = 6, AD_UB = 11;
constexpr int AD_MARGIN = 28, AD_AHEAD = 15;
constexpr int AD_WAIT = 4;
constexpr int QDELAY = 40, QSLEEP = 4, HDELAY = 60, FSLEEP = 3;
constexpr int H1DELAY = 20, MLSLEEP = 4, H2DELAY = 40;
constexpr int TMASK8 = 12;

constexpr unsigned WS_EPOCH = 0;
constexpr unsigned WS_QG = 4096;
constexpr unsigned WS_ML = WS_QG + 8 * 4 * 512 * 8;
constexpr unsigned WS_AG = WS_ML + 16 * 32 * 16;
constexpr unsigned WS_OG = WS_AG + 256 * 4096;
constexpr unsigned WS_FG = WS_OG + NRED * NCON * 64;
constexpr unsigned WS_HG = WS_FG + NRED * NFFN * 64;
constexpr unsigned WS_PG = WS_HG + 28 * 4 * 64 * 8;
constexpr unsigned WS_END = WS_PG + 256 * 16;

constexpr unsigned L_Q = 131072;
constexpr unsigned L_XN = L_Q + 1024;
constexpr unsigned L_WA = L_XN + 896;
constexpr unsigned L_WB = L_WA + 14336;
constexpr unsigned L_O = L_WB + 14336;
constexpr unsigned L_RED = L_O + 64;
constexpr unsigned L_FX = L_RED + 256;
constexpr unsigned L_TAG = L_FX + 64;
constexpr unsigned L_SCHED = L_TAG + 16;
constexpr unsigned L_END = L_SCHED + 16;

__device__ __forceinline__ float bflo(unsigned w) { return __uint_as_float(w << 16); }
__device__ __forceinline__ float bfhi(unsigned w) { return __uint_as_float(w & 0xffff0000u); }
__device__ __forceinline__ float rbf(float f) { return (float)(__bf16)f; }
__device__ __forceinline__ unsigned pk2(float a, float b) { bf16x2 v = {(__bf16)a, (__bf16)b}; return __builtin_bit_cast(unsigned, v); }
template <int CTRL> __device__ __forceinline__ float dpp(float x) {
    return __builtin_bit_cast(float, __builtin_amdgcn_mov_dpp(__builtin_bit_cast(int, x), CTRL, 0xf, 0xf, true));
}
constexpr int XOR1 = 0xB1, XOR2 = 0x4E, ROR4 = 0x124, ROR8 = 0x128;
__device__ __forceinline__ float sum16(float x) {
    auto s = __builtin_amdgcn_permlane16_swap(__float_as_uint(x), __float_as_uint(x), false, false);
    return __uint_as_float(s[0]) + __uint_as_float(s[1]);
}
__device__ __forceinline__ float sum32(float x) {
    auto s = __builtin_amdgcn_permlane32_swap(__float_as_uint(x), __float_as_uint(x), false, false);
    return __uint_as_float(s[0]) + __uint_as_float(s[1]);
}
__device__ __forceinline__ float max16(float x) {
    auto s = __builtin_amdgcn_permlane16_swap(__float_as_uint(x), __float_as_uint(x), false, false);
    return fmaxf(__uint_as_float(s[0]), __uint_as_float(s[1]));
}
__device__ __forceinline__ float max32(float x) {
    auto s = __builtin_amdgcn_permlane32_swap(__float_as_uint(x), __float_as_uint(x), false, false);
    return fmaxf(__uint_as_float(s[0]), __uint_as_float(s[1]));
}
__device__ __forceinline__ float wave_sum(float x) {
    x += dpp<XOR1>(x); x += dpp<XOR2>(x); x += dpp<ROR4>(x); x += dpp<ROR8>(x);
    return sum32(sum16(x));
}
__device__ __forceinline__ float ex2(float x) { return __builtin_amdgcn_exp2f(x); }

#define WG_BARRIER() do { asm volatile("s_waitcnt lgkmcnt(0)" ::: "memory"); __builtin_amdgcn_s_barrier(); asm volatile("" ::: "memory"); } while (0)
#define VMCNT(n) asm volatile("s_waitcnt vmcnt(%0)" :: "i"(n) : "memory")
#define SB() __builtin_amdgcn_sched_barrier(0)

__device__ __forceinline__ void dma(unsigned ldsaddr, unsigned voff, const void* gbase) {
    asm volatile("s_mov_b32 m0, %0\n\ts_nop 0\n\tglobal_load_lds_dwordx4 %1, %2 nt" :: "s"(ldsaddr), "v"(voff), "s"(gbase) : "memory");
}
template <int N> __device__ __forceinline__ void dma_win(unsigned ldsaddr, unsigned voff, const void* gbase) {
    asm volatile("s_waitcnt vmcnt(%3)\n\ts_mov_b32 m0, %0\n\ts_nop 0\n\tglobal_load_lds_dwordx4 %1, %2 nt" :: "s"(ldsaddr), "v"(voff), "s"(gbase), "i"(N - 1) : "memory");
}
template <int N, int X> __device__ __forceinline__ void dma_win3(unsigned ldsaddr, unsigned voff, const void* gbase, unsigned level) {
    asm volatile("s_cmp_gt_u32 %4, 1\n\ts_cbranch_scc1 2f\n\ts_cmp_eq_u32 %4, 1\n\ts_cbranch_scc1 1f\n\ts_waitcnt vmcnt(%6)\n1:\n\ts_waitcnt vmcnt(%3)\n2:\n\ts_waitcnt vmcnt(%5)\n\t"
                 "s_mov_b32 m0, %0\n\ts_nop 0\n\tglobal_load_lds_dwordx4 %1, %2 nt"
                 :: "s"(ldsaddr), "v"(voff), "s"(gbase), "i"(N - 1), "s"(level), "i"(N + X - 1), "i"(N - 2) : "memory", "scc");
}

typedef __amdgpu_buffer_rsrc_t rsrc_t;
__device__ __forceinline__ v4u ld16_sc1(rsrc_t r, unsigned off) { return __builtin_amdgcn_raw_buffer_load_b128(r, off, 0, 16); }
__device__ __forceinline__ void st16_sc1(rsrc_t r, unsigned off, v4u v) { __builtin_amdgcn_raw_buffer_store_b128(v, r, off, 0, 16); }

__device__ __forceinline__ void publish_pair(rsrc_t r, unsigned off, float v, unsigned tag, int lane) {
    const float nb = dpp<XOR1>(v);
    if (!(lane & 1)) st16_sc1(r, off, v4u{__float_as_uint(v), tag, __float_as_uint(nb), tag});
}
template <int N, int SLEEP> __device__ __forceinline__ void gather(rsrc_t r, const unsigned (&off)[N], unsigned tag, v4u (&v)[N]) {
    for (unsigned spin = 0; spin < SPIN_CAP; ++spin) {
        asm volatile("" ::: "memory");
        bool ok = true;
#pragma unroll
        for (int k = 0; k < N; ++k) v[k] = ld16_sc1(r, off[k]);
#pragma unroll
        for (int k = 0; k < N; ++k) ok &= (v[k].y == tag) & (v[k].w == tag);
        if (__all(ok)) break;
        __builtin_amdgcn_s_sleep(SLEEP);
    }
}

__device__ __forceinline__ void layernorm_to_lds(v4u xv, LAS char* xn, int lane) {
    float f[8];
#pragma unroll
    for (int k = 0; k < 4; ++k) { f[2 * k] = bflo(xv[k]); f[2 * k + 1] = bfhi(xv[k]); }
    float s = 0.f;
#pragma unroll
    for (int k = 0; k < 8; ++k) s += f[k];
    const float mean = wave_sum(s) * (1.0f / D);
    float v = 0.f;
#pragma unroll
    for (int k = 0; k < 8; ++k) { const float d = f[k] - mean; v = fmaf(d, d, v); }
    if (lane >= 56) v = 0.f;
    const float var = wave_sum(v) * (1.0f / D);
    const float mb = rbf(mean), den = rbf(sqrtf(rbf(rbf(var) + 1e-5f)));
    v4u o;
#pragma unroll
    for (int k = 0; k < 4; ++k) o[k] = pk2(rbf(f[2 * k] - mb) / den, rbf(f[2 * k + 1] - mb) / den);
    if (lane < 56) *(LAS v4u*)(xn + lane * 16) = o;
}

__device__ __forceinline__ float pick4(int g, float a, float b, float c, float d) { return g == 0 ? a : g == 1 ? b : g == 2 ? c : d; }
__device__ __forceinline__ void block_dma(const u16* w, int ld, unsigned ldsaddr, int lane) {
    const unsigned voff = (unsigned)(lane >> 3) * (ld * 2) + (lane & 7) * 16;
#pragma unroll
    for (int i = 0; i < 14; ++i) dma(ldsaddr + i * 1024, voff, (const char*)w + (size_t)i * 8 * ld * 2);
}
__device__ __forceinline__ float block_mfma(LAS char* L, unsigned wofs, int rb, int lane) {
    const int g = lane >> 4, i = lane & 15;
    v4f d[4] = {{0.f, 0.f, 0.f, 0.f}, {0.f, 0.f, 0.f, 0.f}, {0.f, 0.f, 0.f, 0.f}, {0.f, 0.f, 0.f, 0.f}};
    LAS char* ap = L + L_XN + rb * 224 + g * 8;
    LAS char* bp = L + wofs + (4 * g + (i >> 2)) * 128 + (i & 3) * 8;
#pragma unroll
    for (int ks = 0; ks < 7; ++ks) {
        const v4s av = *(const LAS v4s*)(ap + ks * 32);
#pragma unroll
        for (int t = 0; t < 4; ++t) {
            const v4s bv = __builtin_amdgcn_ds_read_tr16_b64_v4i16((LAS v4s*)(bp + ks * 2048 + t * 32));
            d[t] = __builtin_amdgcn_mfma_f32_16x16x16bf16_1k(av, bv, d[t], 0, 0, 0);
        }
    }
    return pick4(g, d[0][0], d[1][0], d[2][0], d[3][0]);
}

__global__ void __launch_bounds__(NTHR, 2)
fused_decode_kernel(const u16* __restrict__ x, const u16* __restrict__ Kc, const u16* __restrict__ Vc, const u16* __restrict__ wq,
                    const u16* __restrict__ wo, const u16* __restrict__ w1, const u16* __restrict__ w2, u16* __restrict__ out,
                    unsigned char* __restrict__ ws) {
    __shared__ __attribute__((aligned(16))) char lds[L_END];
    LAS char* L = (LAS char*)lds;
    asm volatile("" :: "s"(x), "s"(Kc), "s"(Vc), "s"(wq), "s"(wo), "s"(w1), "s"(w2), "s"(out), "s"(ws));
    const int tid = threadIdx.x, lane = tid & 63, wave = __builtin_amdgcn_readfirstlane(tid >> 6);
    const int b = blockIdx.x, h = b & 7, s = b >> 3;
    const int g = lane >> 4, i16 = lane & 15;
    const unsigned Lb = __builtin_amdgcn_readfirstlane((unsigned)(size_t)L);
    const rsrc_t rws = __builtin_amdgcn_make_buffer_rsrc(ws, 0, WS_END, 0x00020000);
    const bool is_ffn = s < 14, is_con = s >= 16, is_red = b < NRED;
    const int j2 = h * 16 + (s - 16);
    unsigned tag = 0;

    if (wave < NSW) {
        const unsigned ring = Lb + wave * 32768;
        const int hf = lane >> 5, cp = lane & 31;
        unsigned voffK[8], voffV[8];
#pragma unroll
        for (int p = 0; p < 8; ++p) { const int key = 2 * p + hf; voffK[p] = key * 512 + ((cp ^ (key & 15)) * 16); voffV[p] = key * 512 + ((cp ^ (4 * (key & 3))) * 16); }
        const char* Kh = (const char*)Kc + (size_t)h * SK * 512 + (size_t)s * (SK / NSPLIT * 512) + (size_t)wave * 8192;
        const char* Vh = (const char*)Vc + (size_t)h * SK * 512 + (size_t)s * (SK / NSPLIT * 512) + (size_t)wave * 8192;
        constexpr size_t TSTRIDE = (size_t)NSW * 8192;
        const int tmask = (s & 8) ? TMASK8 : 0;
#pragma unroll
        for (int t = 0; t < 4; ++t)
#pragma unroll
            for (int p = 0; p < 8; ++p) dma_win<NFLY - 1>(ring + t * 8192 + p * 1024, voffK[p], Kh + (size_t)(t ^ tmask) * TSTRIDE);
        WG_BARRIER();
        tag = *(LAS unsigned*)(L + L_TAG);
        v8bf qf[8];
#pragma unroll
        for (int ks = 0; ks < 8; ++ks) qf[ks] = *(const LAS v8bf*)(L + L_Q + (i16 & 1) * 512 + ks * 64 + g * 16);
        LAS char* R = L + wave * 32768;
        unsigned ka[4], va[2];
#pragma unroll
        for (int k4 = 0; k4 < 4; ++k4) ka[k4] = i16 * 512 + ((k4 ^ (i16 >> 2)) * 64) + ((g ^ (i16 & 3)) * 16);
        const int q4 = (lane >> 2) & 3, p4 = lane & 3;
#pragma unroll
        for (int u1 = 0; u1 < 2; ++u1) va[u1] = q4 * 512 + (((8 * u1 + 2 * g + (p4 >> 1)) ^ (4 * q4)) * 16) + (p4 & 1) * 8;
        v2u sc[TILES];
        float mx = -INFINITY, m = 0.f, l = 0.f;
        v4f acc[4] = {{0.f, 0.f, 0.f, 0.f}, {0.f, 0.f, 0.f, 0.f}, {0.f, 0.f, 0.f, 0.f}, {0.f, 0.f, 0.f, 0.f}};
        unsigned level = 0, t_a = 0;

#define PIECE(u, p) do { if ((u) >= 1 && (u) <= 28) { \
            if ((u) + 3 < TILES) dma_win3<NFLY, BOOST_X>(ring + (((u) + 3) & 3) * 8192 + (p) * 1024, voffK[p], gb, level); \
            else dma_win3<NFLY, BOOST_X>(ring + (((u) + 3) & 3) * 8192 + (p) * 1024, voffV[p], gb, level); } SB(); } while (0)
#define PACE(u) do { if ((u) >= 1 && (u) <= 28) { \
            if ((u) == AD_UA || (u) >= AD_UB) { const unsigned now_ = (unsigned)__builtin_amdgcn_s_memrealtime(); \
                if ((u) == AD_UA) t_a = now_; \
                if ((u) == AD_UB && wave == 0 && lane == 0) st16_sc1(rws, WS_PG + b * 16, v4u{t_a, tag, now_, tag}); \
                if ((u) > AD_UB) { const v4u sc_ = *(const LAS v4u*)(L + L_SCHED); \
                    const int lag_ = (int)(now_ - (sc_.x + ((((u) - AD_UB) * sc_.y) >> 4))); \
                    level = __builtin_amdgcn_readfirstlane(sc_.z != tag ? 0u : lag_ > 0 ? 2u : lag_ < -AD_AHEAD ? 0u : 1u); } } } } while (0)
#define STEP_BASE(u) const char* gb = ((u) + 3 < TILES ? Kh + (size_t)(((u) + 3) ^ tmask) * TSTRIDE : Vh + (size_t)((((u) + 3 - TILES) & 15) ^ tmask) * TSTRIDE)
#define LANDED(u) asm volatile("s_waitcnt vmcnt(%0) lgkmcnt(0)" :: "i"((u) == 0 ? 24 : (u) <= 29 ? 16 : (u) == 30 ? 8 : 0) : "memory")
#define SOFTMAX(t) do { \
            const float p0 = ex2((bflo(sc[t].x) - m) * SC2), p1 = ex2((bfhi(sc[t].x) - m) * SC2), p2 = ex2((bflo(sc[t].y) - m) * SC2), p3 = ex2((bfhi(sc[t].y) - m) * SC2); \
            l += (p0 + p1) + (p2 + p3); \
            sc[t] = v2u{pk2(p0, p1), pk2(p2, p3)}; } while (0)
#define PHASE_K(u) do { \
            PACE(u); LANDED(u); STEP_BASE(u); \
            v8bf kf[8]; \
            _Pragma("unroll") for (int ks = 0; ks < 8; ++ks) kf[ks] = *(const LAS v8bf*)(R + ((u) & 3) * 8192 + (ks >> 2) * 256 + ka[ks & 3]); \
            SB(); PIECE(u, 0); \
            v4f d = {0.f, 0.f, 0.f, 0.f}; \
            _Pragma("unroll") for (int ks = 0; ks < 4; ++ks) d = __builtin_amdgcn_mfma_f32_16x16x32_bf16(kf[ks], qf[ks], d, 0, 0, 0); \
            SB(); PIECE(u, 1); \
            _Pragma("unroll") for (int ks = 4; ks < 8; ++ks) d = __builtin_amdgcn_mfma_f32_16x16x32_bf16(kf[ks], qf[ks], d, 0, 0, 0); \
            SB(); PIECE(u, 2); \
            sc[u] = v2u{pk2(d[0], d[1]), pk2(d[2], d[3])}; \
            mx = fmaxf(fmaxf(mx, fmaxf(d[0], d[1])), fmaxf(d[2], d[3])); \
            SB(); PIECE(u, 3); \
            if ((u) == TILES - 1) { m = max32(max16(rbf(mx))); SB(); PIECE(u, 4); SOFTMAX(0); SB(); } else PIECE(u, 4); \
            PIECE(u, 5); PIECE(u, 6); PIECE(u, 7); \
        } while (0)
#define PV_RD(kap, u) _Pragma("unroll") for (int c = 0; c < 4; ++c) \
            vf[kap][c] = __builtin_amdgcn_ds_read_tr16_b64_v4i16((LAS v4s*)(R + ((u) & 3) * 8192 + (kap) * 2048 + (c >> 1) * 256 + va[c & 1]));
#define PV_MM(kap) _Pragma("unroll") for (int c = 0; c < 4; ++c) acc[c] = __builtin_amdgcn_mfma_f32_4x4x4bf16_1k(P, vf[kap][c], acc[c], 4, 4 * (kap), 0);
#define PHASE_V(u) do { \
            PACE(u); LANDED(u); STEP_BASE(u); \
            const v4s P = __builtin_bit_cast(v4s, sc[(u) - TILES]); \
            v4s vf[4][4]; \
            PV_RD(0, u) PV_RD(1, u) SB(); PIECE(u, 0); \
            PV_RD(2, u) PV_RD(3, u) SB(); PIECE(u, 1); \
            PV_MM(0) SB(); PIECE(u, 2); \
            PV_MM(1) SB(); PIECE(u, 3); \
            PV_MM(2) SB(); PIECE(u, 4); \
            PV_MM(3) SB(); PIECE(u, 5); \
            if ((u) < 31) { SOFTMAX(((u) - TILES + 1) & 15); SB(); } \
            PIECE(u, 6); PIECE(u, 7); \
        } while (0)
        PHASE_K(0); PHASE_K(1); PHASE_K(2); PHASE_K(3); PHASE_K(4); PHASE_K(5); PHASE_K(6); PHASE_K(7);
        PHASE_K(8); PHASE_K(9); PHASE_K(10); PHASE_K(11); PHASE_K(12); PHASE_K(13); PHASE_K(14); PHASE_K(15);
        PHASE_V(16); PHASE_V(17); PHASE_V(18); PHASE_V(19); PHASE_V(20); PHASE_V(21); PHASE_V(22); PHASE_V(23);
        PHASE_V(24); PHASE_V(25); PHASE_V(26); PHASE_V(27); PHASE_V(28); PHASE_V(29); PHASE_V(30); PHASE_V(31);
        l = sum32(sum16(l));
        LAS float* part = (LAS float*)R;
#pragma unroll
        for (int u = 0; u < 4; ++u) { part[64 * u + lane] = acc[u][0]; part[256 + 64 * u + lane] = acc[u][1]; }
        if (lane < 2) { part[512 + 2 * lane] = m; part[513 + 2 * lane] = l; }
    } else {
        if (wave == 4) {
            const int rb = s & 3, cb = s >> 2;
            v4u xv; unsigned ep;
            const u16* xp = x + (lane < 56 ? lane : 55) * 8;
            asm volatile("global_load_dwordx4 %0, %1, off" : "=v"(xv) : "v"(xp) : "memory");
            asm volatile("global_load_dword %0, %1, off sc1" : "=v"(ep) : "v"(ws + WS_EPOCH) : "memory");
            block_dma(wq + (size_t)rb * 112 * NQ + h * 512 + cb * 64, NQ, Lb + L_WA, lane);
            asm volatile("s_waitcnt vmcnt(15)" : "+v"(xv) :: "memory");
            if (lane >= 56) xv = v4u{0u, 0u, 0u, 0u};
            layernorm_to_lds(xv, L + L_XN, lane);
            asm volatile("s_waitcnt vmcnt(0) lgkmcnt(0)" : "+v"(ep) :: "memory");
            tag = ep + 1u;
            publish_pair(rws, WS_QG + (unsigned)((h * 4 + rb) * 512 + cb * 64 + lane) * 8, block_mfma(L, L_WA, rb, lane), tag, lane);
            unsigned off[16];
#pragma unroll
            for (int k = 0; k < 4; ++k)
#pragma unroll
                for (int r = 0; r < 4; ++r) off[4 * k + r] = WS_QG + (unsigned)((h * 4 + r) * 512 + 128 * k + 2 * lane) * 8;
            v4u v[16];
            __builtin_amdgcn_s_sleep(QDELAY);
            gather<16, QSLEEP>(rws, off, tag, v);
#pragma unroll
            for (int k = 0; k < 4; ++k) {
                const float q0 = (__uint_as_float(v[4 * k].x) + __uint_as_float(v[4 * k + 1].x)) + (__uint_as_float(v[4 * k + 2].x) + __uint_as_float(v[4 * k + 3].x));
                const float q1 = (__uint_as_float(v[4 * k].z) + __uint_as_float(v[4 * k + 1].z)) + (__uint_as_float(v[4 * k + 2].z) + __uint_as_float(v[4 * k + 3].z));
                *(LAS unsigned*)(L + L_Q + (64 * k + lane) * 4) = pk2(q0, q1);
            }
            if (lane == 0) *(LAS unsigned*)(L + L_TAG) = tag;
        }
        WG_BARRIER();
        tag = *(LAS unsigned*)(L + L_TAG);
        if (wave == 5 && is_ffn) {
            const int fs = h * 14 + s, cb = fs >> 2, rb = fs & 3;
            block_dma(w1 + (size_t)rb * 112 * DFF + cb * 64, DFF, Lb + L_WB, lane);
#pragma unroll
            for (int p = 0; p < 14; ++p) dma(Lb + L_WA + p * 1024, lane * 16, (const char*)(w2 + (size_t)fs * 16 * D) + p * 1024);
            VMCNT(14);
            publish_pair(rws, WS_HG + (unsigned)((cb * 4 + rb) * 64 + lane) * 8, block_mfma(L, L_WB, rb, lane), tag, lane);
            const unsigned offh[1] = {WS_HG + (unsigned)((cb * 4 + ((lane >> 3) & 3)) * 64 + 16 * rb + 2 * (lane & 7)) * 8};
            v4u vh[1];
            __builtin_amdgcn_s_sleep(HDELAY);
            gather<1, 16>(rws, offh, tag, vh);
            float h0 = __uint_as_float(vh[0].x), h1 = __uint_as_float(vh[0].z);
            h0 += dpp<ROR8>(h0); h1 += dpp<ROR8>(h1); h0 = sum16(h0); h1 = sum16(h1);
            if (lane < 8) *(LAS unsigned*)(L + L_O + lane * 4) = pk2(fmaxf(rbf(h0), 0.f), fmaxf(rbf(h1), 0.f));
            asm volatile("s_waitcnt vmcnt(0) lgkmcnt(0)" ::: "memory");
            const v4s ha = *(const LAS v4s*)(L + L_O + g * 8);
            LAS char* bp = L + L_WA + (4 * g + (i16 >> 2)) * (D * 2) + (i16 & 3) * 8;
#pragma unroll
            for (int rr = 0; rr < 7; ++rr) {
                float dd[4];
#pragma unroll
                for (int t = 0; t < 4; ++t) {
                    const v4s bv = __builtin_amdgcn_ds_read_tr16_b64_v4i16((LAS v4s*)(bp + (4 * rr + t) * 32));
                    const v4f z = {0.f, 0.f, 0.f, 0.f};
                    dd[t] = __builtin_amdgcn_mfma_f32_16x16x16bf16_1k(ha, bv, z, 0, 0, 0)[0];
                }
                const int c = 64 * rr + lane;
                publish_pair(rws, WS_FG + (unsigned)((c >> 3) * NFFN + fs) * 64 + (c & 7) * 8, pick4(g, dd[0], dd[1], dd[2], dd[3]), tag, lane);
            }
        }
        if (wave == 6 && is_con) {
#pragma unroll
            for (int p = 0; p < 28; ++p) dma(Lb + L_WA + p * 1024, lane * 16, (const char*)(wo + (size_t)j2 * 32 * D) + p * 1024);
            VMCNT(0);
        }
        if (wave == 6 && is_red) {
            for (int z = 0; z < FSLEEP; ++z) __builtin_amdgcn_s_sleep(127);
            unsigned off[7];
#pragma unroll
            for (int k = 0; k < 7; ++k) off[k] = WS_FG + (unsigned)b * NFFN * 64 + k * 1024 + lane * 16;
            v4u v[7];
            gather<7, 64>(rws, off, tag, v);
            float s0 = 0.f, s1 = 0.f;
#pragma unroll
            for (int k = 0; k < 7; ++k) { s0 += __uint_as_float(v[k].x); s1 += __uint_as_float(v[k].z); }
            s0 += dpp<ROR4>(s0); s0 += dpp<ROR8>(s0); s0 = sum32(sum16(s0));
            s1 += dpp<ROR4>(s1); s1 += dpp<ROR8>(s1); s1 = sum32(sum16(s1));
            if (lane < 4) {
                LAS float* fx = (LAS float*)(L + L_FX);
                fx[2 * lane] = rbf(s0); fx[2 * lane + 1] = rbf(s1);
                const unsigned xw = *(const GAS unsigned*)(x + b * 8 + 2 * lane);
                fx[8 + 2 * lane] = bflo(xw); fx[8 + 2 * lane + 1] = bfhi(xw);
            }
        }
        if (wave == 7) {
            for (int z = 0; z < AD_WAIT; ++z) __builtin_amdgcn_s_sleep(127);
            const unsigned offp[1] = {WS_PG + (unsigned)((b & 3) * 64 + lane) * 16};
            v4u vp[1];
            gather<1, 48>(rws, offp, tag, vp);
            const unsigned ref = (unsigned)__builtin_amdgcn_s_memrealtime();
            const float ma = wave_sum((float)(int)(vp[0].x - ref)) * (1.f / 64), mb = wave_sum((float)(int)(vp[0].z - ref)) * (1.f / 64);
            if (lane == 0) *(LAS v4u*)(L + L_SCHED) = v4u{ref + (int)mb + AD_MARGIN, (unsigned)((mb - ma) * (16.f / (AD_UB - AD_UA))), tag, 0u};
        }
    }
    WG_BARRIER();
    if (wave < NSW) {
        const int hh = tid >> 7, d = (tid & 127) * 2;
        float mw[NSW], lw[NSW], a0[NSW], a1[NSW];
#pragma unroll
        for (int w = 0; w < NSW; ++w) {
            const LAS float* part = (const LAS float*)(L + w * 32768);
            const v2u ml = *(const LAS v2u*)(part + 512 + 2 * hh), a2 = *(const LAS v2u*)(part + hh * 256 + d);
            mw[w] = __uint_as_float(ml.x); lw[w] = __uint_as_float(ml.y); a0[w] = __uint_as_float(a2.x); a1[w] = __uint_as_float(a2.y);
        }
        const float M = fmaxf(fmaxf(mw[0], mw[1]), fmaxf(mw[2], mw[3]));
        float A0 = 0.f, A1 = 0.f, Ls = 0.f;
#pragma unroll
        for (int w = 0; w < NSW; ++w) { const float f = ex2((mw[w] - M) * SC2); A0 = fmaf(f, a0[w], A0); A1 = fmaf(f, a1[w], A1); Ls = fmaf(f, lw[w], Ls); }
        const int hq = 2 * h + hh;
        if (d == 0) st16_sc1(rws, WS_ML + (unsigned)(hq * 32 + s) * 16, v4u{__float_as_uint(M), tag, __float_as_uint(Ls), tag});
        st16_sc1(rws, WS_AG + (unsigned)(((hq * 16 + (d >> 4)) * 32 + s) * 16 + (d & 15)) * 8, v4u{__float_as_uint(A0), tag, __float_as_uint(A1), tag});
    }
    if (is_con) {
        v8s wb[4];
        if (wave < 7) {
            LAS char* bp = L + L_WA + (8 * g + (i16 >> 2)) * (D * 2) + (i16 & 3) * 8 + wave * 128;
#pragma unroll
            for (int t = 0; t < 4; ++t) {
                const v4s b0 = __builtin_amdgcn_ds_read_tr16_b64_v4i16((LAS v4s*)(bp + t * 32));
                const v4s b1 = __builtin_amdgcn_ds_read_tr16_b64_v4i16((LAS v4s*)(bp + t * 32 + 4 * D * 2));
                wb[t] = __builtin_shufflevector(b0, b1, 0, 1, 2, 3, 4, 5, 6, 7);
            }
        }
        if (wave == 4) {
            const unsigned offm[1] = {WS_ML + (unsigned)((j2 >> 3) * 32 + (lane & 31)) * 16};
            v4u vm[1];
            __builtin_amdgcn_s_sleep(H1DELAY);
            gather<1, MLSLEEP>(rws, offm, tag, vm);
            unsigned off[8];
#pragma unroll
            for (int k = 0; k < 8; ++k) off[k] = WS_AG + (unsigned)j2 * 8192 + k * 1024 + lane * 16;
            v4u v[8];
#pragma unroll
            for (int k = 0; k < 8; ++k) v[k] = ld16_sc1(rws, off[k]);
            asm volatile("" ::: "memory");
            const float ms = __uint_as_float(vm[0].x);
            float M = fmaxf(ms, dpp<XOR1>(ms)); M = fmaxf(M, dpp<XOR2>(M)); M = fmaxf(M, dpp<ROR4>(M)); M = max16(fmaxf(M, dpp<ROR8>(M)));
            const float f = ex2((ms - M) * SC2);
            float Ls = f * __uint_as_float(vm[0].z);
            Ls += dpp<XOR1>(Ls); Ls += dpp<XOR2>(Ls); Ls += dpp<ROR4>(Ls); Ls += dpp<ROR8>(Ls); Ls = sum16(Ls);
            const int wn = __float_as_int(f * __builtin_amdgcn_rcpf(Ls));
            float wk[4];
#pragma unroll
            for (int k = 0; k < 4; ++k) wk[k] = __int_as_float(__builtin_amdgcn_ds_bpermute(((lane >> 3) + 8 * k) * 4, wn));
            bool ok = true;
#pragma unroll
            for (int k = 0; k < 8; ++k) ok &= (v[k].y == tag) & (v[k].w == tag);
            if (!__all(ok)) gather<8, 1>(rws, off, tag, v);
#pragma unroll
            for (int c = 0; c < 2; ++c) {
                float o0 = 0.f, o1 = 0.f;
#pragma unroll
                for (int k = 0; k < 4; ++k) { o0 = fmaf(wk[k], __uint_as_float(v[4 * c + k].x), o0); o1 = fmaf(wk[k], __uint_as_float(v[4 * c + k].z), o1); }
                o0 += dpp<ROR8>(o0); o1 += dpp<ROR8>(o1);
                o0 = sum32(sum16(o0)); o1 = sum32(sum16(o1));
                if (lane < 8) *(LAS unsigned*)(L + L_O + c * 32 + lane * 4) = pk2(o0, o1);
            }
        }
        WG_BARRIER();
        if (wave < 7) {
            const v8bf oa = *(const LAS v8bf*)(L + L_O + g * 16);
            float dd[4];
#pragma unroll
            for (int t = 0; t < 4; ++t) { const v4f z = {0.f, 0.f, 0.f, 0.f}; dd[t] = __builtin_amdgcn_mfma_f32_16x16x32_bf16(oa, __builtin_bit_cast(v8bf, wb[t]), z, 0, 0, 0)[0]; }
            const int c = 64 * wave + lane;
            publish_pair(rws, WS_OG + (unsigned)((c >> 3) * NCON + j2) * 64 + (c & 7) * 8, pick4(g, dd[0], dd[1], dd[2], dd[3]), tag, lane);
        }
    }
    if (is_red) {
        const unsigned off[1] = {WS_OG + (unsigned)b * (NCON * 64) + wave * 1024 + lane * 16};
        v4u v[1];
        __builtin_amdgcn_s_sleep(H2DELAY);
        gather<1, 1>(rws, off, tag, v);
        float s0 = __uint_as_float(v[0].x), s1 = __uint_as_float(v[0].z);
        s0 += dpp<ROR4>(s0); s0 += dpp<ROR8>(s0); s0 = sum32(sum16(s0));
        s1 += dpp<ROR4>(s1); s1 += dpp<ROR8>(s1); s1 = sum32(sum16(s1));
        if (lane < 4) { LAS float* red = (LAS float*)(L + L_RED) + wave * 8; red[2 * lane] = s0; red[2 * lane + 1] = s1; }
        WG_BARRIER();
        if (tid < 4) {
            const LAS float* red = (const LAS float*)(L + L_RED);
            float a0 = 0.f, a1 = 0.f;
#pragma unroll
            for (int w = 0; w < 8; ++w) { const v2u r2 = *(const LAS v2u*)(red + w * 8 + 2 * tid); a0 += __uint_as_float(r2.x); a1 += __uint_as_float(r2.y); }
            const LAS float* fx = (const LAS float*)(L + L_FX);
            const float y0 = rbf(fx[8 + 2 * tid] + rbf(a0)) + fx[2 * tid], y1 = rbf(fx[9 + 2 * tid] + rbf(a1)) + fx[2 * tid + 1];
            __hip_atomic_store((GAS unsigned*)(out + b * 8 + 2 * tid), pk2(y0, y1), __ATOMIC_RELAXED, __HIP_MEMORY_SCOPE_AGENT);
        }
        if (b == 0 && tid == 0) __hip_atomic_store((GAS unsigned*)(ws + WS_EPOCH), tag, __ATOMIC_RELAXED, __HIP_MEMORY_SCOPE_AGENT);
    }
}

}

extern "C" void kernel_launch(void* const* d_in, const int* in_sizes, int n_in, void* d_out, int out_size, void* d_ws, size_t ws_size,
                              hipStream_t stream) {
    (void)in_sizes; (void)n_in; (void)out_size; (void)ws_size;
    hipLaunchKernelGGL(fused_decode_kernel, dim3(NWG), dim3(NTHR), 0, stream,
                       (const u16*)d_in[0], (const u16*)d_in[1], (const u16*)d_in[2], (const u16*)d_in[3], (const u16*)d_in[4],
                       (const u16*)d_in[6], (const u16*)d_in[7], (u16*)d_out, (unsigned char*)d_ws);
}
```
